```python
import torch

def _softmax(x, axis=-1):
    return torch.softmax(x, dim=axis)
def _gelu(x):
    return torch.nn.functional.gelu(x, approximate="tanh")
def _silu(x):
    return torch.nn.functional.silu(x)
def _sigmoid(x):
    return torch.sigmoid(x)
def _layernorm(x, axis=-1, eps=1e-5):
    m = x.mean(dim=axis, keepdim=True)
    v = x.var(dim=axis, keepdim=True, unbiased=False)
    return (x - m) / torch.sqrt(v + eps)
def _scatter_add(tgt, idx, src, axis=0):
    return tgt.clone().index_add_(axis, idx, src)
def _segment_sum(src, seg, n):
    out = torch.zeros((n,) + src.shape[1:], dtype=src.dtype)
    return out.index_add_(0, seg, src)
def _pad(x, pw):
    flat = tuple(v for pair in reversed(pw) for v in pair)
    return torch.nn.functional.pad(x, flat)

kernel_name = 'standalone__deep_cross_pnn__recsys'

def setup_inputs():

    _w = (lambda a: (lambda t: t.to(torch.bfloat16) if t.is_floating_point() else t)(torch.as_tensor(a)))
    B, F, D, V = 4096, 100, 128, min(1000, 100000)
    H = 4 * D
    return {
        "idx": _w((lambda lo, hi, *s: torch.randint(lo, hi, s))(0, V, B, F).to(torch.int64)),
        "table": _w((lambda *s: torch.randn(*s, dtype=torch.bfloat16) * 1.6)(V, D) * (D ** -0.5)),
        "w_p": _w((lambda *s: torch.randn(*s, dtype=torch.bfloat16) * 0.05)(F * F) * ((F * F) ** -0.5)),
        "w1": _w((lambda *s: torch.randn(*s, dtype=torch.bfloat16) * 1.6)(F * D, H) * ((F * D) ** -0.5)),
        "b1": _w((lambda *s: torch.randn(*s, dtype=torch.bfloat16) * 6.4)(H) * 0.01),
        "w2": _w((lambda *s: torch.randn(*s, dtype=torch.bfloat16) * 0.05)(H) * (H ** -0.5)),
        "b2": _w((lambda *s: torch.randn(*s, dtype=torch.bfloat16) * 0.8)(1) * 0.01),
    }

def reference(idx, table, w_p, w1, b1, w2, b2):
    emb = table[idx]
    B = emb.shape[0]; F = emb.shape[1]; D = emb.shape[2]
    ip = torch.einsum('bid,bjd->bij', emb, emb)
    ip = ip.reshape(B, F * F)
    logit_p = ip @ w_p
    z = emb.reshape(B, F * D)
    h = _gelu(z @ w1 + b1)
    logit_d = h @ w2 + b2
    return logit_p + logit_d
```

```cpp
#include <hip/hip_runtime.h>
#include <hip/hip_bf16.h>
#include <stdint.h>

typedef short bf16x8 __attribute__((ext_vector_type(8)));
typedef float f32x4 __attribute__((ext_vector_type(4)));
typedef float f32x2 __attribute__((ext_vector_type(2)));
typedef __bf16 bf16x2 __attribute__((ext_vector_type(2)));
typedef unsigned u32x4 __attribute__((ext_vector_type(4)));
typedef unsigned u32x2 __attribute__((ext_vector_type(2)));
typedef unsigned short u16;

#define GDEP 6
constexpr int NB = 4096, NF = 100, ND = 128, NV = 1000, NH = 512;

constexpr size_t WS_LIST = 0;
constexpr size_t WS_LX   = WS_LIST + 12 * 256 * 256;
constexpr size_t WS_LP   = WS_LX + 4 * 4096 * 2;
constexpr size_t WS_X    = 1 << 20;

constexpr int L_P  = 0;
constexpr int L_PX = 1008 * 128;
constexpr int L_WB = L_PX + 1008 * 16;
constexpr int L_WX = L_WB + 16384;
constexpr int L_MISC = L_WX + 2048;
constexpr int L_TOTAL = L_MISC + 256;
constexpr int L_WT = L_PX;
static_assert(L_TOTAL == 163840, "lds");

#define MFMA(a, b, c) __builtin_amdgcn_mfma_f32_16x16x32_bf16(__builtin_bit_cast(bf16x8, a), __builtin_bit_cast(bf16x8, b), c, 0, 0, 0)
#define BAR() asm volatile("s_waitcnt lgkmcnt(0)\n\ts_barrier" ::: "memory")
#define SCHED() __builtin_amdgcn_sched_barrier(0)

typedef __amdgpu_buffer_rsrc_t rsrc_t;
__device__ __forceinline__ rsrc_t mkrs(const void* p, unsigned bytes) {
  return __builtin_amdgcn_make_buffer_rsrc((void*)p, 0, bytes, 0x00020000);
}
#define BLD128(rs, vo, so) __builtin_bit_cast(u32x4, __builtin_amdgcn_raw_buffer_load_b128(rs, vo, so, 0))
#define OPAQUE(x) asm volatile("" : "+v"(x))
__device__ __forceinline__ unsigned pack2(float a, float b) {
  f32x2 v = {a, b};
  return __builtin_bit_cast(unsigned, __builtin_convertvector(v, bf16x2));
}
__device__ __forceinline__ float bf2f(unsigned x16) { return __uint_as_float(x16 << 16); }
__device__ __forceinline__ float rbf(float x) { return __uint_as_float(pack2(x, 0.f) << 16); }

__global__ void __launch_bounds__(256) k_prep(const int* __restrict__ idx, char* __restrict__ ws) {
  const int i = blockIdx.x, t = threadIdx.x;
  const int* my = idx + i * 16 * NF;
  if (t < 192) {
    const int fp = t >> 4, e = t & 15, c7 = e & 7, qh = e >> 3;
    const int fbase = fp * 8;
    unsigned o[4];
#pragma unroll
    for (int n = 0; n < 8; n += 2) {
      const int tp = n >> 2, t0 = n & 3;
      const int j0 = (c7 + 4 * qh + t0) & 7, j1 = (c7 + 4 * qh + t0 + 1) & 7;
      const unsigned a0 = (unsigned)my[(8 * tp + c7) * NF + fbase + j0] * 8 + j0;
      const unsigned a1 = (unsigned)my[(8 * tp + c7) * NF + fbase + j1] * 8 + j1;
      o[n >> 1] = a0 | (a1 << 16);
    }
    *(u32x4*)(ws + WS_LIST + ((size_t)fp * 256 + i) * 256 + e * 16) = (u32x4){o[0], o[1], o[2], o[3]};
  } else {
    const int u = t - 192, phi = u >> 4, s = u & 15;
    ((u16*)(ws + WS_LX))[phi * 4096 + i * 16 + s] = (u16)my[s * NF + 96 + phi];
  }
}

__device__ __forceinline__ int fresh_lane() {
  int l;
  asm volatile("v_mbcnt_lo_u32_b32 %0, -1, 0\n\tv_mbcnt_hi_u32_b32 %0, -1, %0" : "=v"(l));
  return l;
}
__device__ __forceinline__ u32x4 make_sel(int c, int q) {
  u32x4 sel;
  const int e = c - 8 * (q & 1);
#pragma unroll
  for (int k = 0; k < 4; ++k) sel[k] = (e == 2 * k) ? 0x3F80u : (e == 2 * k + 1) ? 0x3F800000u : 0u;
  return sel;
}
__device__ __forceinline__ int w_src(int lane, int w) {
  const int jp = lane & 3, sf = (lane >> 2) & 7, sq = (lane >> 5) + 2 * (w & 1), ss = w >> 1;
  return (sf * 128 + 32 * ss + 8 * sq + 2 * jp) * 1024;
}
__device__ __forceinline__ void stage_w(char* smem, int lane, int w, u32x4 r0, u32x4 r1) {
  const int jp = lane & 3, sf = (lane >> 2) & 7, sq = (lane >> 5) + 2 * (w & 1), ss = w >> 1;
  const int wdst = L_WB + (ss * 64 + sq * 16 + 2 * sf) * 16 + jp * 4;
#pragma unroll
  for (int h = 0; h < 8; ++h) {
    const unsigned d = __builtin_amdgcn_perm(r1[h >> 1], r0[h >> 1], (h & 1) ? 0x07060302u : 0x05040100u);
    *(unsigned*)(smem + wdst + (h & 3) * 4096 + (h >> 2) * 16) = d;
  }
}
__device__ __forceinline__ int list_off(int lane) {
  const int c = lane & 15, q = lane >> 4;
  return ((q & 1) * 2 + (c >> 3)) * 256 + ((q >> 1) * 8 + (c & 7)) * 16;
}

__global__ void __launch_bounds__(512, 2)
k_main(const int* __restrict__ idx, const char* __restrict__ table, const u16* __restrict__ wp,
       const char* __restrict__ w1, char* __restrict__ ws) {
  __shared__ __attribute__((aligned(16))) char smem[L_TOTAL];
  const int w = __builtin_amdgcn_readfirstlane(threadIdx.x >> 6);
  const int bid = blockIdx.x, xl = bid & 7, rr = bid >> 3;
  const int g = xl * 8 + (rr & 7), phi = rr >> 3;
  const rsrc_t rs_w1 = mkrs(w1, NF * ND * NH * 2), rs_tab = mkrs(table, NV * ND * 2), rs_ws = mkrs(ws, (unsigned)WS_X);
  const int wsb = phi * 24 * 128 * 1024 + g * 16;
  const int lsb = (int)WS_LIST + (phi * 3 * 256 + w * 32) * 256;

  u32x4 T[8][4];
  {
    const int lane = fresh_lane(), c = lane & 15, q = lane >> 4;
    const int wsrc = w_src(lane, w);
    const u32x4 wp0 = BLD128(rs_w1, wsrc, wsb), wp1 = BLD128(rs_w1, wsrc + 1024, wsb);
    u32x4 xr0, xr1;
    if (w == 0) {
      xr0 = BLD128(rs_w1, lane * 2048, (96 + phi) * 128 * 1024 + g * 16);
      xr1 = BLD128(rs_w1, lane * 2048 + 1024, (96 + phi) * 128 * 1024 + g * 16);
    }
#pragma unroll
    for (int m = 0; m < 8; ++m)
#pragma unroll
      for (int s = 0; s < 4; ++s) T[m][s] = BLD128(rs_tab, c * 256 + 16 * q + 64 * s, (128 * w + 16 * m) * 256);
    stage_w(smem, lane, w, wp0, wp1);
    if (w == 0) {
      const int xd = L_WX + (lane >> 2) * 128 + (lane & 3) * 4;
#pragma unroll
      for (int h = 0; h < 8; ++h) {
        const unsigned d = __builtin_amdgcn_perm(xr1[h >> 1], xr0[h >> 1], (h & 1) ? 0x07060302u : 0x05040100u);
        *(unsigned*)(smem + xd + h * 16) = d;
      }
    }
  }
  f32x4 acc[16];
#pragma unroll
  for (int i = 0; i < 16; ++i) acc[i] = (f32x4){0.f, 0.f, 0.f, 0.f};
  BAR();

#pragma unroll 1
  for (int ph = 0; ph < 3; ++ph) {
    u32x4 ix[2];
    if (ph == 1) {
      const int lane = fresh_lane(), c = lane & 15, q = lane >> 4;
      const u32x4 sel = make_sel(c, q);
      const u32x4 slo = q < 2 ? sel : (u32x4){0, 0, 0, 0}, shi = q < 2 ? (u32x4){0, 0, 0, 0} : sel;
      const int lo = list_off(lane) >> 3;
      unsigned a[8];
#pragma unroll
      for (int n = 0; n < 8; ++n) a[n] = __builtin_amdgcn_raw_buffer_load_b16(rs_ws, lo + n * 128, (int)WS_LX + phi * 8192 + w * 1024, 0);
#pragma unroll
      for (int n = 0; n < 8; ++n) {
        const u32x4 gv = *(const u32x4*)(smem + L_PX + a[n] * 16);
        acc[2 * n] = MFMA(slo, gv, acc[2 * n]);
        acc[2 * n + 1] = MFMA(shi, gv, acc[2 * n + 1]);
      }
    }
    {
      const int lane = fresh_lane(), c = lane & 15, q = lane >> 4;
      const int pw = L_P + (128 * w + 4 * q) * 128 + c * 8;
#pragma unroll
      for (int half = 0; half < 2; ++half) {
        u32x4 Wf[2][4];
#pragma unroll
        for (int t = 0; t < 2; ++t)
#pragma unroll
          for (int s = 0; s < 4; ++s) Wf[t][s] = *(const u32x4*)(smem + L_WB + ((2 * half + t) * 4 + s) * 1024 + lane * 16);
        if (half == 1) {
          const int lsrc = list_off(lane);
          ix[0] = BLD128(rs_ws, lsrc, lsb + ph * 65536);
          ix[1] = BLD128(rs_ws, lsrc + 1024, lsb + ph * 65536);
        }
#pragma unroll
        for (int m = 0; m < 8; ++m) {
          if (m < 7 || w < 7) {
            f32x4 d0 = {0.f, 0.f, 0.f, 0.f}, d1 = {0.f, 0.f, 0.f, 0.f};
#pragma unroll
            for (int s = 0; s < 4; ++s) { d0 = MFMA(T[m][s], Wf[0][s], d0); d1 = MFMA(T[m][s], Wf[1][s], d1); }
#pragma unroll
            for (int r = 0; r < 4; ++r) *(unsigned*)(smem + pw + (16 * m + r) * 128 + half * 4) = pack2(d0[r], d1[r]);
          }
        }
      }
    }
    BAR();
    if (ph == 0) {
      const int lane = fresh_lane(), c = lane & 15, q = lane >> 4;
      u32x4 Wx[4];
#pragma unroll
      for (int s = 0; s < 4; ++s) Wx[s] = *(const u32x4*)(smem + L_WX + (s * 4 + q) * 128 + (c & 7) * 16);
      const int xw = L_PX + (128 * w + c) * 16 + (q & 1) * 8;
#pragma unroll
      for (int m = 0; m < 8; ++m) {
        if (m < 7 || w < 7) {
          f32x4 d = {0.f, 0.f, 0.f, 0.f};
#pragma unroll
          for (int s = 0; s < 4; ++s) d = MFMA(Wx[s], T[m][s], d);
          *(u32x2*)(smem + xw + m * 256) = (u32x2){pack2(d[0], d[1]), pack2(d[2], d[3])};
        }
      }
    }
    {
      const int lane = fresh_lane(), c = lane & 15, q = lane >> 4;
      const int wsrc = w_src(lane, w);
      const u32x4 wr0 = BLD128(rs_w1, wsrc, wsb + (ph + 1) * 8 * 128 * 1024), wr1 = BLD128(rs_w1, wsrc + 1024, wsb + (ph + 1) * 8 * 128 * 1024);
      const u32x4 sel = make_sel(c, q);
      const int lsrc = list_off(lane);
      constexpr int DEP = GDEP;
      u32x4 gv[DEP];
      auto gaddr = [&](int n) -> unsigned {
        const unsigned x = ix[(n >> 3) & 1][(n >> 1) & 3];
        return ((n & 1) ? (x >> 16) : (x & 0xffffu)) << 4;
      };
#pragma unroll
      for (int n = 0; n < DEP; ++n) gv[n] = *(const u32x4*)(smem + L_P + gaddr(n));
#pragma unroll
      for (int n = 0; n < 64; ++n) {
        acc[n >> 2] = MFMA(sel, gv[n % DEP], acc[n >> 2]);
        if (n + DEP < 64) gv[n % DEP] = *(const u32x4*)(smem + L_P + gaddr(n + DEP));
        if (((n + DEP) & 7) == 7 && ((n + DEP) >> 3) + 2 < 8)
          ix[((n + DEP) >> 3) & 1] = BLD128(rs_ws, lsrc, lsb + ph * 65536 + (((n + DEP) >> 3) + 2) * 1024);
        SCHED();
      }
      stage_w(smem, fresh_lane(), w, wr0, wr1);
    }
    BAR();
  }

  const int lane = fresh_lane(), c = lane & 15, q = lane >> 4;
  {
    char* xd = ws + WS_X + ((size_t)(w * 32 + (q >> 1) * 2 + (c >> 3)) * 256 + (g * 4 + phi)) * 256 + ((q & 1) * 8 + (c & 7)) * 16;
#pragma unroll
    for (int ch = 0; ch < 8; ++ch) {
      const f32x4 a = acc[2 * ch], b = acc[2 * ch + 1];
      *(u32x4*)(xd + (size_t)ch * 4 * 65536) = (u32x4){pack2(a[0], a[1]), pack2(a[2], a[3]), pack2(b[0], b[1]), pack2(b[2], b[3])};
    }
  }

  {
    const rsrc_t rs_wp = mkrs(wp, NF * NF * 2);
#pragma unroll 1
    for (int tile = w; tile < 28; tile += 8) {
      int ti = 0, rem = tile;
      while (rem >= 7 - ti) { rem -= 7 - ti; ++ti; }
      const int tj = ti + rem;
      const int j = 16 * tj + c, i0 = 16 * ti + 4 * q;
      const bool ok = (j < NF) && (i0 < NF);
      const int oa = ok ? (i0 * NF + j) * 2 : 0x100000, ob = (ok && ti != tj) ? (j * NF + i0) * 2 : 0x100000;
      const u32x2 tb = __builtin_bit_cast(u32x2, __builtin_amdgcn_raw_buffer_load_b64(rs_wp, ob, 0, 0));
      f32x4 o;
#pragma unroll
      for (int r = 0; r < 4; ++r) {
        const unsigned a = __builtin_amdgcn_raw_buffer_load_b16(rs_wp, oa + r * NF * 2, 0, 0);
        o[r] = bf2f(a) + ((r & 1) ? __uint_as_float(tb[r >> 1] & 0xffff0000u) : bf2f(tb[r >> 1]));
      }
      *(f32x4*)(smem + L_WT + tile * 1024 + lane * 16) = o;
    }
  }
  BAR();
#pragma unroll 1
  for (int sm = 0; sm < 2; ++sm) {
    const int b = bid * 16 + w * 2 + sm;
    const int lane = fresh_lane(), c = lane & 15, q = lane >> 4;
    u32x4 E[7][4];
#pragma unroll
    for (int t = 0; t < 7; ++t) {
      int f = 16 * t + c; f = f < NF ? f : NF - 1;
      const int v = idx[b * NF + f];
#pragma unroll
      for (int s = 0; s < 4; ++s) E[t][s] = BLD128(rs_tab, v * 256 + 16 * q + 64 * s, 0);
    }
    float sum = 0.f;
    int tile = 0;
#pragma unroll
    for (int ti = 0; ti < 7; ++ti)
#pragma unroll
      for (int tj = ti; tj < 7; ++tj, ++tile) {
        f32x4 d = {0.f, 0.f, 0.f, 0.f};
#pragma unroll
        for (int s = 0; s < 4; ++s) d = MFMA(E[ti][s], E[tj][s], d);
        const f32x4 wt = *(const f32x4*)(smem + L_WT + tile * 1024 + lane * 16);
        sum += d[0] * wt[0] + d[1] * wt[1] + d[2] * wt[2] + d[3] * wt[3];
      }
#pragma unroll
    for (int o = 32; o >= 1; o >>= 1) sum += __shfl_xor(sum, o);
    if (lane == 0) ((float*)(ws + WS_LP))[b] = sum;
  }
}

__device__ __forceinline__ float gelu_tanh(float x) {
  const float u = 0.7978845608028654f * (x + 0.044715f * x * x * x);
  const float e = __expf(-2.f * u);
  return x * __builtin_amdgcn_rcpf(1.f + e);
}

__global__ void __launch_bounds__(512)
k_final(const u16* __restrict__ b1, const u16* __restrict__ w2, const u16* __restrict__ b2,
        const char* __restrict__ ws, u16* __restrict__ out) {
  __shared__ float red[8][16];
  const int tid = threadIdx.x, lane = tid & 63, w = tid >> 6, i = blockIdx.x;
  const int e = lane & 15, gl = lane >> 4;
  const char* src = ws + WS_X + ((size_t)i * 256 + (8 * w + gl) * 4) * 256 + e * 16;
  u32x4 x[8];
#pragma unroll
  for (int k = 0; k < 8; ++k) x[k] = *(const u32x4*)(src + (k >> 2) * 4096 + (k & 3) * 256);
  float s0 = 0.f, s1 = 0.f;
#pragma unroll
  for (int kk = 0; kk < 2; ++kk) {
    const int col = (8 * w + 4 * kk + gl) * 8 + (e >> 3) * 4;
    const u32x2 bb = *(const u32x2*)(b1 + col), ww = *(const u32x2*)(w2 + col);
#pragma unroll
    for (int r = 0; r < 4; ++r) {
      const float bv = (r & 1) ? __uint_as_float(bb[r >> 1] & 0xffff0000u) : bf2f(bb[r >> 1]);
      const float wv = (r & 1) ? __uint_as_float(ww[r >> 1] & 0xffff0000u) : bf2f(ww[r >> 1]);
#pragma unroll
      for (int tp = 0; tp < 2; ++tp) {
        float z = 0.f;
#pragma unroll
        for (int ph = 0; ph < 4; ++ph) {
          const unsigned d = x[kk * 4 + ph][tp * 2 + (r >> 1)];
          z += (r & 1) ? __uint_as_float(d & 0xffff0000u) : bf2f(d);
        }
        const float h = rbf(gelu_tanh(rbf(rbf(z) + bv)));
        if (tp) s1 += h * wv; else s0 += h * wv;
      }
    }
  }
#pragma unroll
  for (int o = 8; o <= 32; o <<= 1) { s0 += __shfl_xor(s0, o); s1 += __shfl_xor(s1, o); }
  if (lane < 8) { red[w][lane] = s0; red[w][8 + lane] = s1; }
  __syncthreads();
  if (tid < 16) {
    float s = 0.f;
#pragma unroll
    for (int k = 0; k < 8; ++k) s += red[k][tid];
    const float ld = rbf(rbf(s) + bf2f(b2[0]));
    const float lp = rbf(((const float*)(ws + WS_LP))[i * 16 + tid]);
    out[i * 16 + tid] = (u16)(pack2(lp + ld, 0.f) & 0xffffu);
  }
}

extern "C" void kernel_launch(void* const* d_in, const int* in_sizes, int n_in, void* d_out, int out_size,
                              void* d_ws, size_t ws_size, hipStream_t stream) {
  const int* idx = (const int*)d_in[0];
  const char* table = (const char*)d_in[1];
  const u16* wp = (const u16*)d_in[2];
  const char* w1 = (const char*)d_in[3];
  const u16* b1 = (const u16*)d_in[4];
  const u16* w2 = (const u16*)d_in[5];
  const u16* b2 = (const u16*)d_in[6];
  char* ws = (char*)d_ws;
  k_prep<<<256, 256, 0, stream>>>(idx, ws);
  k_main<<<256, 512, 0, stream>>>(idx, table, wp, w1, ws);
  k_final<<<256, 512, 0, stream>>>(b1, w2, b2, ws, (u16*)d_out);
}
```

```cpp
#include <hip/hip_runtime.h>
#include <hip/hip_bf16.h>
#include <stdint.h>

typedef short bf16x8 __attribute__((ext_vector_type(8)));
typedef float f32x4 __attribute__((ext_vector_type(4)));
typedef float f32x2 __attribute__((ext_vector_type(2)));
typedef __bf16 bf16x2 __attribute__((ext_vector_type(2)));
typedef unsigned u32x4 __attribute__((ext_vector_type(4)));
typedef unsigned u32x2 __attribute__((ext_vector_type(2)));
typedef unsigned short u16;
typedef unsigned long long u64;
typedef __attribute__((address_space(1))) u64 gu64;
typedef __amdgpu_buffer_rsrc_t rsrc_t;

#define NSTAMP 19
constexpr int NB = 4096, NF = 100, ND = 128, NV = 1000, NH = 512;

constexpr int WS_EPOCH = 0;
constexpr int WS_F1   = 256;
constexpr int WS_F2   = 4096;
constexpr int WS_LX   = 32768;
constexpr int WS_WT   = 65536;
constexpr int WS_LIST = 131072;
constexpr int WS_X    = 1 << 20;
constexpr int WS_END  = WS_X + (16 << 20);
constexpr size_t WS_STAMP = 32u << 20;

#define STAMP(k)
#define RSTAMP(k)

constexpr int L_P  = 0;
constexpr int L_WX = 1008 * 128;
constexpr int L_PX = L_WX + 2048;
constexpr int L_WB = L_PX + 1008 * 16;
constexpr int L_MISC = L_WB + 16384;
constexpr int L_TOTAL = L_MISC + 256;
constexpr int L_WT = L_PX;
constexpr int L_WT6 = L_WT + 21 * 1024;
constexpr int L_RED = L_WX;
static_assert(L_TOTAL == 163840, "lds");

#define MFMA(a, b, c) __builtin_amdgcn_mfma_f32_16x16x32_bf16(__builtin_bit_cast(bf16x8, a), __builtin_bit_cast(bf16x8, b), c, 0, 0, 0)
#define BAR() asm volatile("s_waitcnt lgkmcnt(0)\n\ts_barrier" ::: "memory")
#define SCHED() __builtin_amdgcn_sched_barrier(0)
#define DRAIN() asm volatile("s_waitcnt vmcnt(0)" ::: "memory")
#define RLX_AGENT __ATOMIC_RELAXED, __HIP_MEMORY_SCOPE_AGENT
constexpr int SC1 = 16;
#define BLD128(rs, vo, so) __builtin_bit_cast(u32x4, __builtin_amdgcn_raw_buffer_load_b128(rs, vo, so, 0))
#define BLD128C(rs, vo, so) __builtin_bit_cast(u32x4, __builtin_amdgcn_raw_buffer_load_b128(rs, vo, so, SC1))
#define BST128C(v, rs, vo, so) do { const u32x4 v_ = v; __builtin_amdgcn_raw_buffer_store_b128(v_, rs, vo, so, SC1); asm volatile("s_nop 1" :: "v"(v_)); } while (0)
#define ZERO4 ((f32x4){0.f, 0.f, 0.f, 0.f})

template <int I> struct IC { static constexpr int v = I; };
template <int B, int E, class F> __device__ __forceinline__ void sfor(F&& f) {
  if constexpr (B < E) { f(IC<B>{}); sfor<B + 1, E>(f); }
}
__device__ __forceinline__ rsrc_t mkrs(const void* p, unsigned bytes) {
  return __builtin_amdgcn_make_buffer_rsrc((void*)p, 0, bytes, 0x00020000);
}
__device__ __forceinline__ unsigned pack2(float a, float b) {
  f32x2 v = {a, b};
  return __builtin_bit_cast(unsigned, __builtin_convertvector(v, bf16x2));
}
__device__ __forceinline__ float bf2f(unsigned x16) { return __uint_as_float(x16 << 16); }
__device__ __forceinline__ float bfhi(unsigned x) { return __uint_as_float(x & 0xffff0000u); }
__device__ __forceinline__ float rbf(float x) { return __uint_as_float(pack2(x, 0.f) << 16); }
__device__ __forceinline__ int fresh_lane() {
  unsigned z;
  asm volatile("s_mov_b32 %0, 0" : "=s"(z));
  const int l = (int)__builtin_amdgcn_mbcnt_hi(~0u, __builtin_amdgcn_mbcnt_lo(~0u, z));
  __builtin_assume(l >= 0 && l < 64);
  return l;
}
__device__ __forceinline__ u32x4 make_sel(int c, int q) {
  u32x4 sel;
  const int e = c - 8 * (q & 1);
  const unsigned one = (e & 1) ? 0x3F800000u : 0x3F80u;
#pragma unroll
  for (int k = 0; k < 4; ++k) sel[k] = ((e >> 1) == k) ? one : 0u;
  return sel;
}
__device__ __forceinline__ int w_src(int lane, int w) {
  const int jp = lane & 3, sf = (lane >> 2) & 7, sq = (lane >> 5) + 2 * (w & 1), ss = w >> 1;
  return (sf * 128 + 32 * sq + 8 * ss + 2 * jp) * 1024;
}
__device__ __forceinline__ void stage_w(char* smem, int lane, int w, u32x4 r0, u32x4 r1) {
  const int jp = lane & 3, sf = (lane >> 2) & 7, sq = (lane >> 5) + 2 * (w & 1), ss = w >> 1;
  const int wdst = L_WB + (ss * 64 + sq * 16 + 2 * sf) * 16 + jp * 4;
#pragma unroll
  for (int h = 0; h < 8; ++h) {
    const unsigned d = __builtin_amdgcn_perm(r1[h >> 1], r0[h >> 1], (h & 1) ? 0x07060302u : 0x05040100u);
    *(unsigned*)(smem + wdst + (h & 3) * 4096 + (h >> 2) * 16) = d;
  }
}
__device__ __forceinline__ int list_off(int lane) {
  const int c = lane & 15, q = lane >> 4;
  return ((q & 1) * 2 + (c >> 3)) * 256 + ((q >> 1) * 8 + (c & 7)) * 16;
}
struct QT { u32x4 t0, t2; };
__device__ __forceinline__ void qt_init(QT& t) { asm volatile("s_nop 7" : "=v"(t.t0), "=v"(t.t2)); }
#define CNDX(perm, o, own, oth, m) do { register u64 m_ asm("vcc") = m; unsigned o_ = o; \
  asm volatile("v_cndmask_b32_dpp %0, %2, %1, vcc quad_perm:" perm " row_mask:0xf bank_mask:0xf" : "+v"(o_) : "v"(own), "v"(oth), "s"(m_)); o = o_; } while (0)
#define CNDXI(perm, own, oth, m) do { register u64 m_ asm("vcc") = m; unsigned o_ = own; \
  asm volatile("v_cndmask_b32_dpp %0, %1, %0, vcc quad_perm:" perm " row_mask:0xf bank_mask:0xf" : "+v"(o_) : "v"(oth), "s"(m_)); own = o_; } while (0)
__device__ __forceinline__ void qt_step(u32x4 (&x)[4], QT& t, int n) {
  const int st = n >> 3, p = (n >> 2) & 1, d = n & 3;
  if (st == 0) {
    if (p == 0) CNDX("[1,0,3,2]", t.t0[d], x[0][d], x[1][d], 0x5555555555555555ull);
    else        CNDX("[1,0,3,2]", t.t2[d], x[2][d], x[3][d], 0x5555555555555555ull);
  } else if (st == 1) {
    if (p == 0) CNDXI("[1,0,3,2]", x[3][d], x[2][d], 0xAAAAAAAAAAAAAAAAull);
    else        CNDXI("[1,0,3,2]", x[1][d], x[0][d], 0xAAAAAAAAAAAAAAAAull);
  } else if (st == 2) {
    if (p == 0) CNDX("[2,3,0,1]", x[0][d], t.t0[d], t.t2[d], 0x3333333333333333ull);
    else        CNDX("[2,3,0,1]", x[2][d], x[1][d], x[3][d], 0x3333333333333333ull);
  } else {
    if (p == 0) CNDXI("[2,3,0,1]", t.t2[d], t.t0[d], 0xCCCCCCCCCCCCCCCCull);
    else        CNDXI("[2,3,0,1]", x[3][d], x[1][d], 0xCCCCCCCCCCCCCCCCull);
  }
  if (n == 31) { const u32x4 dead = x[1]; x[1] = x[2]; x[2] = t.t2; t.t2 = dead; asm volatile("s_nop 1"); SCHED(); }
}
__device__ __forceinline__ void qt_all(u32x4 (&x)[4], QT& t) {
#pragma unroll
  for (int n = 0; n < 32; ++n) qt_step(x, t, n);
}
__device__ __forceinline__ void wait_flags(const char* p, u64 tag) {
  gu64* f = (gu64*)(p + (fresh_lane() & 31) * 8);
#pragma unroll 1
  for (int spins = 0; spins < (1 << 16); ++spins) {
    const u64 v = __hip_atomic_load(f, RLX_AGENT);
    if (__all(v == tag)) break;
    __builtin_amdgcn_s_sleep(2);
  }
  __builtin_amdgcn_fence(__ATOMIC_ACQUIRE, "wavefront");
}
__device__ __forceinline__ float gelu_tanh(float x) {
  const float u = 0.7978845608028654f * (x + 0.044715f * x * x * x);
  const float e = __expf(-2.f * u);
  return x * __builtin_amdgcn_rcpf(1.f + e);
}

#define IXD 5
#define TPRE 3
#define GDEP 4

__global__ void __launch_bounds__(512, 2)
k_fused(const int* __restrict__ idx, const char* __restrict__ table, const u16* __restrict__ wp,
        const char* __restrict__ w1, const u16* __restrict__ b1, const u16* __restrict__ w2,
        const u16* __restrict__ b2, char* __restrict__ ws, u16* __restrict__ out) {
  __shared__ __attribute__((aligned(16))) char smem[L_TOTAL];
  const int w = __builtin_amdgcn_readfirstlane(threadIdx.x >> 6);
  const int bid = blockIdx.x, xl = bid & 7, rr = bid >> 3;
  const int g = xl * 8 + (rr & 7), phi = rr >> 3;
  const rsrc_t rs_w1 = mkrs(w1, NF * ND * NH * 2), rs_tab = mkrs(table, NV * ND * 2), rs_ws = mkrs(ws, WS_END);
  const rsrc_t rs_idx = mkrs(idx, NB * NF * 4);
  const int wsb = phi * 24 * 128 * 1024 + g * 16;
  const int lsb = WS_LIST + (phi * 3 * 256 + w * 32) * 256;
  unsigned stamps = 0;
  RSTAMP(62); STAMP(0);

  u64 tag;
  u32x4 T[8][4];
  auto load_t = [&](int m, int i, int toff) { T[m][i] = BLD128(rs_tab, toff + i * 256, (128 * w + 16 * m) * 256); };
  {
    const int lane = fresh_lane();
    __builtin_amdgcn_s_setprio(3);
    const int wsrc = w_src(lane, w);
    const u32x4 wp0 = BLD128(rs_w1, wsrc, wsb), wp1 = BLD128(rs_w1, wsrc + 1024, wsb);
    const u64 ep = __hip_atomic_load((gu64*)(ws + WS_EPOCH), RLX_AGENT);
    u32x4 xr0, xr1;
    if (w == 0) {
      xr0 = BLD128(rs_w1, lane * 2048, (96 + phi) * 128 * 1024 + g * 16);
      xr1 = BLD128(rs_w1, lane * 2048 + 1024, (96 + phi) * 128 * 1024 + g * 16);
      if (lane == 0) *(unsigned*)(smem + L_MISC) = 0u;
    }
    if (w == 7) {
      u32x4 pi[7];
#pragma unroll
      for (int k = 0; k < 7; ++k) pi[k] = BLD128(rs_idx, lane * 16 + k * 1024, bid * 16 * NF * 4);
      {
        const rsrc_t rs_wp = mkrs(wp, NF * NF * 2);
        const int id = bid * 23 + lane, tile = id >> 8, l = (id >> 2) & 63, r = id & 3, c = l & 15, q = l >> 4;
        int tj = (tile >= 1) + (tile >= 3) + (tile >= 6) + (tile >= 10) + (tile >= 15);
        int i = 16 * (tile - tj * (tj + 1) / 2) + 4 * q + r, j = 16 * tj + c;
        bool oka = true, okb = (i >> 4) != tj;
        if (tile >= 21) {
          tj = (tile - 21) * 4 + r; i = 96 + q; j = tj < 6 ? 16 * tj + c : 96 + (c >> 2);
          oka = tj < 6 || (tj == 6 && (c & 3) == 0); okb = tj < 6;
        }
        const unsigned a = __builtin_amdgcn_raw_buffer_load_b16(rs_wp, (oka && lane < 23) ? (i * NF + j) * 2 : 0x100000, 0, 0);
        const unsigned b = __builtin_amdgcn_raw_buffer_load_b16(rs_wp, (okb && lane < 23) ? (j * NF + i) * 2 : 0x100000, 0, 0);
        __builtin_amdgcn_raw_buffer_store_b32(__float_as_uint(bf2f(a) + bf2f(b)), rs_ws, lane < 23 ? lane * 4 : 0x7f000000, WS_WT + bid * 92, SC1);
      }
#pragma unroll
      for (int k = 0; k < 7; ++k) *(u32x4*)(smem + L_PX + lane * 16 + k * 1024) = pi[k];
      const int e = lane & 15, c7 = e & 7, qh = e >> 3;
#pragma unroll
      for (int k = 0; k < 3; ++k) {
        const int fp = (lane >> 4) + 4 * k;
        u32x4 o;
#pragma unroll
        for (int n = 0; n < 8; n += 2) {
          const int tp = n >> 2, t0 = n & 3;
          const int j0 = (c7 + 4 * qh + t0) & 7, j1 = (c7 + 4 * qh + t0 + 1) & 7;
          const unsigned a0 = *(const unsigned*)(smem + L_PX + ((8 * tp + c7) * NF + fp * 8 + j0) * 4) * 8 + j0;
          const unsigned a1 = *(const unsigned*)(smem + L_PX + ((8 * tp + c7) * NF + fp * 8 + j1) * 4) * 8 + j1;
          o[n >> 1] = a0 | (a1 << 16);
        }
        BST128C(o, rs_ws, fp * 65536 + e * 16, WS_LIST + bid * 256);
      }
      {
        const unsigned v = *(const unsigned*)(smem + L_PX + (e * NF + 96 + (lane >> 4)) * 4);
        const int rl = (qh * 2 + ((bid >> 1) & 1)) * 16 + (bid & 1) * 8 + c7;
        __builtin_amdgcn_raw_buffer_store_b16((u16)v, rs_ws, (lane >> 4) * 8192 + rl * 16, WS_LX + (bid >> 5) * 1024 + ((bid >> 2) & 7) * 2, SC1);
      }
    } else {
      __builtin_amdgcn_s_setprio(0);
    }
    {
      const int toff = ((lane >> 2) & 3) * 1024 + (lane >> 4) * 64 + (lane & 3) * 16;
#pragma unroll
      for (int m = 0; m < TPRE; ++m)
#pragma unroll
        for (int i = 0; i < 4; ++i) load_t(m, i, toff);
    }
    __builtin_amdgcn_s_setprio(0);
    stage_w(smem, lane, w, wp0, wp1);
    if (w == 0) {
      const int xd = L_WX + (((lane >> 2) & 3) * 4 + (lane >> 4)) * 128 + (lane & 3) * 4;
#pragma unroll
      for (int h = 0; h < 8; ++h) {
        const unsigned d = __builtin_amdgcn_perm(xr1[h >> 1], xr0[h >> 1], (h & 1) ? 0x07060302u : 0x05040100u);
        *(unsigned*)(smem + xd + h * 16) = d;
      }
    }
    const unsigned e0 = __builtin_amdgcn_readfirstlane((unsigned)ep), e1 = __builtin_amdgcn_readfirstlane((unsigned)(ep >> 32));
    tag = (((u64)e1 << 32) | e0) * 6364136223846793005ull + 1442695040888963407ull;
  }
  STAMP(1);
  BAR();
  if (w == 7) {
    DRAIN();
    if (fresh_lane() == 0) __hip_atomic_store((gu64*)(ws + WS_F1 + bid * 8), tag, RLX_AGENT);
  }
  STAMP(2);
  auto touch_w = [&](int ph) -> unsigned {
    const int lane = fresh_lane();
    return __builtin_amdgcn_raw_buffer_load_b32(rs_w1, lane < 16 ? (w * 16 + lane) * 1024 : 0x7f000000, wsb + (ph * 8 + (rr & 7)) * 128 * 1024, 0);
  };
  auto touch_l = [&](int ph) -> unsigned {
    const int lane = fresh_lane();
    return __builtin_amdgcn_raw_buffer_load_b32(rs_ws, lane < 8 ? ((rr & 7) * 8 + lane) * 128 : 0x7f000000, lsb + ph * 65536, SC1);
  };

  u32x4 ix[IXD];
  auto load_ix = [&](int ph) {
    const int lsrc = list_off(fresh_lane());
#pragma unroll
    for (int k = 0; k < IXD; ++k) ix[k] = BLD128C(rs_ws, lsrc + (k & 3) * 1024, lsb + ph * 65536 + (k >> 2) * 4096);
  };
  {
    const int lane = fresh_lane(), c = lane & 15, q = lane >> 4;
    const int pw = L_P + (128 * w + 4 * q) * 128 + c * 8;
    const int pw7 = (w == 7) ? L_P + (1000 + 4 * (q & 1)) * 128 + c * 8 : pw + 7 * 2048;
    u32x4 Wf[4][4];
#pragma unroll
    for (int t = 0; t < 4; ++t)
#pragma unroll
      for (int s = 0; s < 4; ++s) Wf[t][s] = *(const u32x4*)(smem + L_WB + (t * 4 + s) * 1024 + lane * 16);
    const int toff = ((lane >> 2) & 3) * 1024 + (lane >> 4) * 64 + (lane & 3) * 16;
    unsigned tw;
    QT qt; qt_init(qt);
    qt_all(T[0], qt);
    f32x4 D[2][4];
#pragma unroll
    for (int m = 0; m <= 8; ++m) {
#pragma unroll
      for (int k = 0; k < 16; ++k) {
        const int s = k >> 2, t = k & 3;
        if (m < 8) D[m & 1][t] = MFMA(T[m][s], Wf[t][s], s == 0 ? ZERO4 : D[m & 1][t]);
        if (m < 7) { qt_step(T[m + 1], qt, 2 * k); qt_step(T[m + 1], qt, 2 * k + 1); }
        if (m + TPRE < 8 && (k & 3) == 1) load_t(m + TPRE, k >> 2, toff);
        if (m + TPRE == 8 && k == 1) tw = touch_w(1);
        if (m > 0 && k >= 8 && (k & 1) == 0) {
          const int r = (k - 8) >> 1;
          const f32x4* d = D[(m - 1) & 1];
          *(u32x2*)(smem + ((m - 1) == 7 ? pw7 : pw + (m - 1) * 2048) + r * 128) = (u32x2){pack2(d[0][r], d[1][r]), pack2(d[2][r], d[3][r])};
        }
        SCHED();
      }
    }
    wait_flags(ws + WS_F1 + w * 256, tag);
    asm volatile("" :: "v"(tw));
    load_ix(0);
  }
  f32x4 acc[16];
#pragma unroll
  for (int i = 0; i < 16; ++i) acc[i] = ZERO4;
  STAMP(3);
  BAR();
  STAMP(4);

  auto extra_compute = [&]() {
    const int lane = fresh_lane(), c = lane & 15, q = lane >> 4;
    u32x4 Wx[4];
#pragma unroll
    for (int s = 0; s < 4; ++s) Wx[s] = *(const u32x4*)(smem + L_WX + (s * 4 + q) * 128 + (c & 7) * 16);
    const int xw = L_PX + (128 * w + c) * 16 + (q & 1) * 8;
    const int xw7 = (w == 7) ? L_PX + (1000 + (c & 7)) * 16 + (q & 1) * 8 : xw + 7 * 256;
#pragma unroll
    for (int m = 0; m < 8; m += 2) {
      f32x4 d0 = ZERO4, d1 = ZERO4;
#pragma unroll
      for (int s = 0; s < 4; ++s) { d0 = MFMA(Wx[s], T[m][s], d0); d1 = MFMA(Wx[s], T[m + 1][s], d1); }
      *(u32x2*)(smem + xw + m * 256) = (u32x2){pack2(d0[0], d0[1]), pack2(d0[2], d0[3])};
      *(u32x2*)(smem + (m == 6 ? xw7 : xw + (m + 1) * 256)) = (u32x2){pack2(d1[0], d1[1]), pack2(d1[2], d1[3])};
    }
  };

#pragma unroll 1
  for (int ph = 0; ph < 2; ++ph) {
    u32x4 lx;
    {
      if (ph == 0 && w >= 4) extra_compute();
      const int lane = fresh_lane(), c = lane & 15, q = lane >> 4;
      const int lsrc = list_off(lane);
      u32x4 wr0, wr1;
      const u32x4 sel = make_sel(c, q);
      constexpr int DEP = GDEP;
      u32x4 gv[DEP];
      auto gaddr = [&](int n) -> unsigned {
        const unsigned x = ix[(n >> 3) % IXD][(n >> 1) & 3];
        return ((n & 1) ? (x >> 16) : (x & 0xffffu)) << 4;
      };
#pragma unroll
      for (int n = 0; n < DEP; ++n) gv[n] = *(const u32x4*)(smem + L_P + gaddr(n));
#pragma unroll
      for (int n = 0; n < 64; ++n) {
        acc[n >> 2] = MFMA(sel, gv[n % DEP], acc[n >> 2]);
        if (n + DEP < 64) gv[n % DEP] = *(const u32x4*)(smem + L_P + gaddr(n + DEP));
        if (((n + DEP) & 7) == 7) {
          const int k = ((n + DEP) >> 3) + IXD;
          if (k < 8) ix[k % IXD] = BLD128C(rs_ws, lsrc + (k & 3) * 1024, lsb + ph * 65536 + (k >> 2) * 4096);
          if (k == 7) {
            const int wsrc = w_src(fresh_lane(), w);
            wr0 = BLD128(rs_w1, wsrc, wsb + (ph + 1) * 8 * 128 * 1024); wr1 = BLD128(rs_w1, wsrc + 1024, wsb + (ph + 1) * 8 * 128 * 1024);
          }
          if (k == 8) lx = BLD128C(rs_ws, fresh_lane() * 16, WS_LX + phi * 8192 + w * 1024);
        }
        SCHED();
      }
      stage_w(smem, fresh_lane(), w, wr0, wr1);
      if (ph == 0 && w < 4) extra_compute();
    }
    STAMP(5 + ph * 4);
    BAR();
    STAMP(6 + ph * 4);
    auto extra_gather = [&]() {
      const int lane = fresh_lane(), c = lane & 15, q = lane >> 4;
      const u32x4 sel = make_sel(c, q);
      const u32x4 slo = q < 2 ? sel : (u32x4){0, 0, 0, 0}, shi = q < 2 ? (u32x4){0, 0, 0, 0} : sel;
      u32x4 gv[8];
#pragma unroll
      for (int n = 0; n < 8; ++n) gv[n] = *(const u32x4*)(smem + L_PX + (((n & 1) ? (lx[n >> 1] >> 16) : (lx[n >> 1] & 0xffffu)) << 4));
#pragma unroll
      for (int n = 0; n < 8; ++n) {
        acc[2 * n] = MFMA(slo, gv[n], acc[2 * n]);
        acc[2 * n + 1] = MFMA(shi, gv[n], acc[2 * n + 1]);
      }
    };
    if (ph == 0) extra_gather();
    {
      const int lane = fresh_lane(), c = lane & 15, q = lane >> 4;
      int pw0 = L_P + (128 * w + 4 * q) * 128 + c * 8, pw1 = pw0 + 128;
      asm volatile("" : "+v"(pw0), "+v"(pw1));
      u32x4 Wf[2][4];
#pragma unroll
      for (int t = 0; t < 2; ++t)
#pragma unroll
        for (int s = 0; s < 4; ++s) Wf[t][s] = *(const u32x4*)(smem + L_WB + (t * 4 + s) * 1024 + lane * 16);
      f32x4 D[2][2];
      unsigned pk[4], tw, tl;
#pragma unroll
      for (int u = 0; u <= 17; ++u) {
        const int m = u & 7;
#pragma unroll
        for (int s = 0; s < 4; ++s) {
          if (u < 16) {
#pragma unroll
            for (int t = 0; t < 2; ++t) {
              D[u & 1][t] = MFMA(T[m][s], Wf[t][s], s == 0 ? ZERO4 : D[u & 1][t]);
              if (u == 7) { Wf[t][s] = *(const u32x4*)(smem + L_WB + ((2 + t) * 4 + s) * 1024 + lane * 16); SCHED(); }
            }
          }
          if (u >= 2 && s == 0) {
            const int pm = (u - 2) & 7;
            *(unsigned*)(smem + pw0 + pm * 2048) = pk[0];
            *(unsigned*)(smem + pw0 + pm * 2048 + 256) = pk[2];
          }
          if (u >= 2 && s == 2) {
            const int pm = (u - 2) & 7;
            *(unsigned*)(smem + pw1 + pm * 2048) = pk[1];
            *(unsigned*)(smem + pw1 + pm * 2048 + 256) = pk[3];
            if (u == 9) { pw0 += 4; pw1 += 4; asm volatile("" : "+v"(pw0), "+v"(pw1)); }
          }
          SCHED();
        }
        if (u == 2) { tw = touch_w(2); tl = touch_l(ph + 1); }
        if (u == 15) { asm volatile("" :: "v"(tw), "v"(tl)); load_ix(ph + 1); }
        if (u >= 1 && u <= 16) {
          const f32x4* d = D[(u - 1) & 1];
#pragma unroll
          for (int r = 0; r < 4; ++r) pk[r] = pack2(d[0][r], d[1][r]);
        }
        SCHED();
      }
    }
    STAMP(7 + ph * 4);
    BAR();
    STAMP(8 + ph * 4);
  }

  u32x4 E[7][4], E2[7][4];
  u32x4 vi[2][7];
  {
    const int lane = fresh_lane(), c = lane & 15, q = lane >> 4;
    const int lsrc = list_off(lane);
    u32x4 jx[8];
#pragma unroll
    for (int k = 0; k < IXD; ++k) jx[k] = ix[k];
#pragma unroll
    for (int k = IXD; k < 8; ++k) jx[k] = BLD128C(rs_ws, lsrc + (k & 3) * 1024, lsb + 2 * 65536 + (k >> 2) * 4096);
#pragma unroll
    for (int t = 0; t < 6; ++t) vi[0][t] = BLD128(rs_idx, ((lane >> 2) & 3) * 16 + t * 64, (bid * 16 + w * 2) * NF * 4);
    vi[0][6][0] = __builtin_amdgcn_raw_buffer_load_b32(rs_idx, ((lane >> 2) & 3) * 4 + 384, (bid * 16 + w * 2) * NF * 4, 0);
    if (w >= 4) {
      u32x4 wt[6];
#pragma unroll
      for (int k = 0; k < 6; ++k) wt[k] = BLD128C(rs_ws, lane * 16 + (k & 3) * 1024, WS_WT + (w - 4) * 6144 + (k >> 2) * 4096);
#pragma unroll
      for (int k = 0; k < 6; ++k) *(u32x4*)(smem + L_WT + (w - 4) * 6144 + k * 1024 + lane * 16) = wt[k];
      asm volatile("s_waitcnt lgkmcnt(0)" ::: "memory");
      if (lane == 0) __hip_atomic_fetch_add((unsigned*)(smem + L_MISC), 1u, __ATOMIC_RELAXED, __HIP_MEMORY_SCOPE_WORKGROUP);
    }
    const int eoff = (lane >> 4) * 64 + (lane & 3) * 16;
    const u32x4 sel = make_sel(c, q);
    const int xo = ((q >> 1) * 2 + (c >> 3)) * 65536 + ((q & 1) * 8 + (c & 7)) * 16;
    const int xb = WS_X + (w * 32 * 256 + g * 4 + phi) * 256;
    constexpr int DEP = GDEP;
    u32x4 gv[DEP];
    auto gaddr = [&](int n) -> unsigned {
      const unsigned x = jx[n >> 3][(n >> 1) & 3];
      return ((n & 1) ? (x >> 16) : (x & 0xffffu)) << 4;
    };
#pragma unroll
    for (int n = 0; n < DEP; ++n) gv[n] = *(const u32x4*)(smem + L_P + gaddr(n));
#pragma unroll
    for (int n = 0; n < 64 + 4; ++n) {
      if (n < 64) acc[n >> 2] = MFMA(sel, gv[n % DEP], acc[n >> 2]);
      if (n + DEP < 64) gv[n % DEP] = *(const u32x4*)(smem + L_P + gaddr(n + DEP));
      if (n >= 8 && n < 8 + 25) {
        const int k = n - 8, t = k >> 2, i = k & 3;
        E[t][i] = BLD128(rs_tab, vi[0][t][i] * 256 + eoff, 0);
      }
      if (n >= 11 && (n & 7) == 3) {
        const int ch = (n >> 3) - 1;
        const f32x4 a = acc[2 * ch], b = acc[2 * ch + 1];
        BST128C(((u32x4){pack2(a[0], a[1]), pack2(a[2], a[3]), pack2(b[0], b[1]), pack2(b[2], b[3])}), rs_ws, xo, xb + ch * 4 * 65536);
      }
      if (n == 40) {
#pragma unroll
        for (int t = 0; t < 6; ++t) vi[1][t] = BLD128(rs_idx, ((lane >> 2) & 3) * 16 + t * 64, (bid * 16 + w * 2 + 1) * NF * 4);
        vi[1][6][0] = __builtin_amdgcn_raw_buffer_load_b32(rs_idx, ((lane >> 2) & 3) * 4 + 384, (bid * 16 + w * 2 + 1) * NF * 4, 0);
      }
      SCHED();
    }
  }
  STAMP(13);

#pragma unroll 1
  for (int spins = 0; spins < (1 << 20); ++spins) {
    if (__builtin_amdgcn_readfirstlane(__hip_atomic_load((unsigned*)(smem + L_MISC), __ATOMIC_RELAXED, __HIP_MEMORY_SCOPE_WORKGROUP)) == 4u) break;
    __builtin_amdgcn_s_sleep(1);
  }
  asm volatile("" ::: "memory");
  STAMP(14);
  u32x4 X[8];
  u32x2 bb[2], ww[2];
  auto pnn = [&](u32x4 (&Ee)[7][4], auto hook) -> float {
    const int lane = fresh_lane();
    f32x4 sm = ZERO4, D[3], wt[2];
    QT qt; qt_init(qt);
    qt_all(Ee[0], qt);
    sfor<0, 30>([&](auto U) {
      constexpr int u = U.v;
      constexpr int tj = u < 1 ? 0 : u < 3 ? 1 : u < 6 ? 2 : u < 10 ? 3 : u < 15 ? 4 : u < 21 ? 5 : 6, ti = u - tj * (tj + 1) / 2, nm = 4 * (tj + 1);
      if constexpr (u >= 1 && u - 1 < 21) wt[(u - 1) & 1] = *(const f32x4*)(smem + L_WT + (u - 1) * 1024 + lane * 16);
      if constexpr (u - 1 == 21 || u - 1 == 25) wt[((u - 1) >> 2) & 1] = *(const f32x4*)(smem + L_WT6 + ((u - 1 - 21) >> 2) * 1024 + lane * 16);
      sfor<0, 4>([&](auto S) {
        constexpr int s = S.v, k = ti * 4 + s;
        if constexpr (u < 21) D[u % 3] = MFMA(Ee[ti][s], Ee[tj][s], s == 0 ? ZERO4 : D[u % 3]);
        else if constexpr (u < 28) D[u % 3] = MFMA(Ee[6][s], Ee[ti][s], s == 0 ? ZERO4 : D[u % 3]);
        if constexpr (tj < 5) sfor<(k * 32) / nm, ((k + 1) * 32) / nm>([&](auto N) { qt_step(Ee[tj + 1], qt, N.v); });
        if constexpr (tj == 5 && k < 16) {
          constexpr int j = 3 - (k >> 2), d = k & 3;
          Ee[6][j][d] = __builtin_amdgcn_mov_dpp(Ee[6][0][d], j * 0x55, 0xf, 0xf, true);
        }
        if constexpr (u >= 2 && u - 2 < 21) { sm[s] += D[(u - 2) % 3][s] * wt[(u - 2) & 1][s]; asm volatile("" : "+v"(sm[s])); }
        if constexpr (u - 2 >= 21 && s == 0) { sm[0] += D[(u - 2) % 3][0] * wt[(u - 2 < 25) ? 1 : 0][(u - 2 - 21) & 3]; asm volatile("" : "+v"(sm[0])); }
        SCHED();
      });
      if constexpr (ti == tj) hook(tj);
    });
    float sum = (sm[0] + sm[1]) + (sm[2] + sm[3]);
#pragma unroll
    for (int o = 32; o >= 1; o >>= 1) sum += __shfl_xor(sum, o);
    return sum;
  };
  const float lp0 = pnn(E, [&](int tj) {
    if (tj == 3) {
      DRAIN();
      const int lane = fresh_lane();
      if (lane == 0) __hip_atomic_store((gu64*)(ws + WS_F2 + (w * 256 + g * 4 + phi) * 8), tag, RLX_AGENT);
      const int eoff = (lane >> 4) * 64 + (lane & 3) * 16;
#pragma unroll
      for (int k = 0; k < 25; ++k) E2[k >> 2][k & 3] = BLD128(rs_tab, vi[1][k >> 2][k & 3] * 256 + eoff, 0);
    }
  });
  STAMP(15);
  const float lp1 = pnn(E2, [&](int tj) {
    if (tj == 3) {
      const int lane = fresh_lane();
#pragma unroll
      for (int kk = 0; kk < 2; ++kk) {
        const int col = (8 * w + 4 * kk + (lane >> 4)) * 8 + ((lane >> 3) & 1) * 4;
        bb[kk] = *(const u32x2*)(b1 + col); ww[kk] = *(const u32x2*)(w2 + col);
      }
      wait_flags(ws + WS_F2 + ((bid >> 5) * 256 + w * 32) * 8, tag);
#pragma unroll
      for (int k = 0; k < 8; ++k) X[k] = BLD128C(rs_ws, (lane >> 4) * 1024 + (lane & 15) * 16 + (k & 3) * 256, WS_X + (bid * 256 + w * 32) * 256 + (k >> 2) * 4096);
    }
  });
  STAMP(16);

  {
    const int lane = fresh_lane();
    float s0 = 0.f, s1 = 0.f;
#pragma unroll
    for (int kk = 0; kk < 2; ++kk) {
#pragma unroll
      for (int r = 0; r < 4; ++r) {
        const float bv = (r & 1) ? bfhi(bb[kk][r >> 1]) : bf2f(bb[kk][r >> 1]);
        const float wv = (r & 1) ? bfhi(ww[kk][r >> 1]) : bf2f(ww[kk][r >> 1]);
#pragma unroll
        for (int tp = 0; tp < 2; ++tp) {
          float z = 0.f;
#pragma unroll
          for (int p = 0; p < 4; ++p) {
            const unsigned d = X[kk * 4 + p][tp * 2 + (r >> 1)];
            z += (r & 1) ? bfhi(d) : bf2f(d);
          }
          const float h = rbf(gelu_tanh(rbf(rbf(z) + bv)));
          if (tp) s1 += h * wv; else s0 += h * wv;
        }
      }
    }
#pragma unroll
    for (int o = 8; o <= 32; o <<= 1) { s0 += __shfl_xor(s0, o); s1 += __shfl_xor(s1, o); }
    float* red = (float*)(smem + L_RED);
    if (lane < 8) { red[w * 16 + lane] = s0; red[w * 16 + 8 + lane] = s1; }
    if (lane == 0) { red[128 + 2 * w] = lp0; red[128 + 2 * w + 1] = lp1; }
    STAMP(17);
    BAR();
    if (w == 0 && lane < 16) {
      float s = 0.f;
#pragma unroll
      for (int k = 0; k < 8; ++k) s += red[k * 16 + lane];
      const float ld = rbf(rbf(s) + bf2f(b2[0]));
      out[bid * 16 + lane] = (u16)(pack2(rbf(red[128 + lane]) + ld, 0.f) & 0xffffu);
      if (bid == 0 && lane == 0) __hip_atomic_store((gu64*)(ws + WS_EPOCH), tag, RLX_AGENT);
    }
  }
  STAMP(18);
  RSTAMP(63);
}

extern "C" void kernel_launch(void* const* d_in, const int* in_sizes, int n_in, void* d_out, int out_size,
                              void* d_ws, size_t ws_size, hipStream_t stream) {
  k_fused<<<256, 512, 0, stream>>>((const int*)d_in[0], (const char*)d_in[1], (const u16*)d_in[2], (const char*)d_in[3],
                                   (const u16*)d_in[4], (const u16*)d_in[5], (const u16*)d_in[6], (char*)d_ws, (u16*)d_out);
}
```

```cpp
#include <hip/hip_runtime.h>
#include <hip/hip_bf16.h>
#include <stdint.h>

typedef short bf16x8 __attribute__((ext_vector_type(8)));
typedef float f32x4 __attribute__((ext_vector_type(4)));
typedef float f32x2 __attribute__((ext_vector_type(2)));
typedef __bf16 bf16x2 __attribute__((ext_vector_type(2)));
typedef unsigned u32x4 __attribute__((ext_vector_type(4)));
typedef unsigned u32x2 __attribute__((ext_vector_type(2)));
typedef unsigned short u16;
typedef unsigned long long u64;
typedef __attribute__((address_space(1))) u64 gu64;
typedef __amdgpu_buffer_rsrc_t rsrc_t;

#define NSTAMP 19
constexpr int NB = 4096, NF = 100, ND = 128, NV = 1000, NH = 512;

constexpr int WS_EPOCH = 0;
constexpr int WS_F1   = 256;
constexpr int WS_F2   = 4096;
constexpr int WS_LX   = 32768;
constexpr int WS_WT   = 65536;
constexpr int WS_LIST = 131072;
constexpr int WS_X    = 1 << 20;
constexpr int WS_END  = WS_X + (16 << 20);
constexpr size_t WS_STAMP = 32u << 20;

#define STAMP(k)
#define STAMPL(k)
#define RSTAMP(k)

constexpr int L_P  = 0;
constexpr int L_WX = 1008 * 128;
constexpr int L_PX = L_WX + 2048;
constexpr int L_WB = L_PX + 1008 * 16;
constexpr int L_MISC = L_WB + 16384;
constexpr int L_TOTAL = L_MISC + 256;
constexpr int L_WT = L_PX;
constexpr int L_WT6 = L_WT + 21 * 1024;
constexpr int L_RED = L_WX;
static_assert(L_TOTAL == 163840, "lds");

#define MFMA(a, b, c) __builtin_amdgcn_mfma_f32_16x16x32_bf16(__builtin_bit_cast(bf16x8, a), __builtin_bit_cast(bf16x8, b), c, 0, 0, 0)
template <bool FIRST> __device__ __forceinline__ void mf(f32x4& d, const u32x4& a, const u32x4& b) {
  if constexpr (FIRST) asm volatile("v_mfma_f32_16x16x32_bf16 %0, %1, %2, 0" : "+v"(d) : "v"(a), "v"(b));
  else asm volatile("v_mfma_f32_16x16x32_bf16 %0, %1, %2, %0" : "+v"(d) : "v"(a), "v"(b));
}
template <int R> __device__ __forceinline__ void pin1(u32x4& x) {
  if constexpr (R == 16) asm volatile("" : "+{v[64:67]}"(x));
  if constexpr (R == 17) asm volatile("" : "+{v[68:71]}"(x));
  if constexpr (R == 18) asm volatile("" : "+{v[72:75]}"(x));
  if constexpr (R == 19) asm volatile("" : "+{v[76:79]}"(x));
  if constexpr (R == 20) asm volatile("" : "+{v[80:83]}"(x));
  if constexpr (R == 22) asm volatile("" : "+{v[88:91]}"(x));
  if constexpr (R == 21) asm volatile("" : "+{v[84:87]}"(x));
}
template <int OFF> __device__ __forceinline__ void dma16(unsigned vo, int lds, rsrc_t rs, int so) {
  asm volatile("s_mov_b32 m0, %1\n\ts_nop 0\n\tbuffer_load_dwordx4 %0, %2, %3 offen offset:%4 lds" :: "v"(vo), "s"(lds), "s"(rs), "s"(so), "i"(OFF) : "memory");
}
template <int OFF> __device__ __forceinline__ void ld16(u32x4& x, unsigned vo, rsrc_t rs, int so) {
  asm volatile("buffer_load_dwordx4 %0, %1, %2, %3 offen offset:%4" : "=v"(x) : "v"(vo), "s"(rs), "s"(so), "i"(OFF));
}
template <int N> __device__ __forceinline__ void wait4(u32x4 (&x)[4]) {
  asm volatile("s_waitcnt vmcnt(%4)" : "+v"(x[0]), "+v"(x[1]), "+v"(x[2]), "+v"(x[3]) : "i"(N));
}
template <class X> __device__ __forceinline__ void opaque(X& x) { asm volatile("" : "+v"(x)); }
#define RESERVE(x) asm volatile("" : "=v"(x))
template <int I> __device__ __forceinline__ void mfma_acc(f32x4& c, const u32x4& a, const u32x4& b) {
  if constexpr (I == 0) asm volatile("v_mfma_f32_16x16x32_bf16 %0, %1, %2, %0" : "+{v[192:195]}"(c) : "v"(a), "v"(b));
  if constexpr (I == 1) asm volatile("v_mfma_f32_16x16x32_bf16 %0, %1, %2, %0" : "+{v[196:199]}"(c) : "v"(a), "v"(b));
  if constexpr (I == 2) asm volatile("v_mfma_f32_16x16x32_bf16 %0, %1, %2, %0" : "+{v[200:203]}"(c) : "v"(a), "v"(b));
  if constexpr (I == 3) asm volatile("v_mfma_f32_16x16x32_bf16 %0, %1, %2, %0" : "+{v[204:207]}"(c) : "v"(a), "v"(b));
  if constexpr (I == 4) asm volatile("v_mfma_f32_16x16x32_bf16 %0, %1, %2, %0" : "+{v[208:211]}"(c) : "v"(a), "v"(b));
  if constexpr (I == 5) asm volatile("v_mfma_f32_16x16x32_bf16 %0, %1, %2, %0" : "+{v[212:215]}"(c) : "v"(a), "v"(b));
  if constexpr (I == 6) asm volatile("v_mfma_f32_16x16x32_bf16 %0, %1, %2, %0" : "+{v[216:219]}"(c) : "v"(a), "v"(b));
  if constexpr (I == 7) asm volatile("v_mfma_f32_16x16x32_bf16 %0, %1, %2, %0" : "+{v[220:223]}"(c) : "v"(a), "v"(b));
  if constexpr (I == 8) asm volatile("v_mfma_f32_16x16x32_bf16 %0, %1, %2, %0" : "+{v[224:227]}"(c) : "v"(a), "v"(b));
  if constexpr (I == 9) asm volatile("v_mfma_f32_16x16x32_bf16 %0, %1, %2, %0" : "+{v[228:231]}"(c) : "v"(a), "v"(b));
  if constexpr (I == 10) asm volatile("v_mfma_f32_16x16x32_bf16 %0, %1, %2, %0" : "+{v[232:235]}"(c) : "v"(a), "v"(b));
  if constexpr (I == 11) asm volatile("v_mfma_f32_16x16x32_bf16 %0, %1, %2, %0" : "+{v[236:239]}"(c) : "v"(a), "v"(b));
  if constexpr (I == 12) asm volatile("v_mfma_f32_16x16x32_bf16 %0, %1, %2, %0" : "+{v[240:243]}"(c) : "v"(a), "v"(b));
  if constexpr (I == 13) asm volatile("v_mfma_f32_16x16x32_bf16 %0, %1, %2, %0" : "+{v[244:247]}"(c) : "v"(a), "v"(b));
  if constexpr (I == 14) asm volatile("v_mfma_f32_16x16x32_bf16 %0, %1, %2, %0" : "+{v[248:251]}"(c) : "v"(a), "v"(b));
  if constexpr (I == 15) asm volatile("v_mfma_f32_16x16x32_bf16 %0, %1, %2, %0" : "+{v[252:255]}"(c) : "v"(a), "v"(b));
}
template <int M> __device__ __forceinline__ void pin4(u32x4 (&x)[4]) {
  if constexpr (M == 0) asm volatile("" : "+{v[64:67]}"(x[0]), "+{v[68:71]}"(x[1]), "+{v[72:75]}"(x[2]), "+{v[76:79]}"(x[3]));
  if constexpr (M == 1) asm volatile("" : "+{v[80:83]}"(x[0]), "+{v[84:87]}"(x[1]), "+{v[88:91]}"(x[2]), "+{v[92:95]}"(x[3]));
  if constexpr (M == 2) asm volatile("" : "+{v[96:99]}"(x[0]), "+{v[100:103]}"(x[1]), "+{v[104:107]}"(x[2]), "+{v[108:111]}"(x[3]));
  if constexpr (M == 3) asm volatile("" : "+{v[112:115]}"(x[0]), "+{v[116:119]}"(x[1]), "+{v[120:123]}"(x[2]), "+{v[124:127]}"(x[3]));
  if constexpr (M == 4) asm volatile("" : "+{v[128:131]}"(x[0]), "+{v[132:135]}"(x[1]), "+{v[136:139]}"(x[2]), "+{v[140:143]}"(x[3]));
  if constexpr (M == 5) asm volatile("" : "+{v[144:147]}"(x[0]), "+{v[148:151]}"(x[1]), "+{v[152:155]}"(x[2]), "+{v[156:159]}"(x[3]));
  if constexpr (M == 6) asm volatile("" : "+{v[160:163]}"(x[0]), "+{v[164:167]}"(x[1]), "+{v[168:171]}"(x[2]), "+{v[172:175]}"(x[3]));
  if constexpr (M == 7) asm volatile("" : "+{v[176:179]}"(x[0]), "+{v[180:183]}"(x[1]), "+{v[184:187]}"(x[2]), "+{v[188:191]}"(x[3]));
  if constexpr (M == 8) asm volatile("" : "+{v[192:195]}"(x[0]), "+{v[196:199]}"(x[1]), "+{v[200:203]}"(x[2]), "+{v[204:207]}"(x[3]));
  if constexpr (M == 9) asm volatile("" : "+{v[208:211]}"(x[0]), "+{v[212:215]}"(x[1]), "+{v[216:219]}"(x[2]), "+{v[220:223]}"(x[3]));
  if constexpr (M == 10) asm volatile("" : "+{v[224:227]}"(x[0]), "+{v[228:231]}"(x[1]), "+{v[232:235]}"(x[2]), "+{v[236:239]}"(x[3]));
  if constexpr (M == 11) asm volatile("" : "+{v[240:243]}"(x[0]), "+{v[244:247]}"(x[1]), "+{v[248:251]}"(x[2]), "+{v[252:255]}"(x[3]));
}
#define BAR() asm volatile("s_waitcnt lgkmcnt(0)\n\ts_barrier" ::: "memory")
#define SCHED() __builtin_amdgcn_sched_barrier(0)
#define DRAIN() asm volatile("s_waitcnt vmcnt(0)" ::: "memory")
#define RLX_AGENT __ATOMIC_RELAXED, __HIP_MEMORY_SCOPE_AGENT
constexpr int SC1 = 16;
#define BLD128(rs, vo, so) __builtin_bit_cast(u32x4, __builtin_amdgcn_raw_buffer_load_b128(rs, vo, so, 0))
#define BLD128C(rs, vo, so) __builtin_bit_cast(u32x4, __builtin_amdgcn_raw_buffer_load_b128(rs, vo, so, SC1))
#define BST128C(v, rs, vo, so) do { const u32x4 v_ = v; __builtin_amdgcn_raw_buffer_store_b128(v_, rs, vo, so, SC1); asm volatile("s_nop 1" :: "v"(v_)); } while (0)
#define ZERO4 ((f32x4){0.f, 0.f, 0.f, 0.f})

template <int I> struct IC { static constexpr int v = I; };
template <int B, int E, class F> __device__ __forceinline__ void sfor(F&& f) {
  if constexpr (B < E) { f(IC<B>{}); sfor<B + 1, E>(f); }
}
__device__ __forceinline__ rsrc_t mkrs(const void* p, unsigned bytes) {
  return __builtin_amdgcn_make_buffer_rsrc((void*)p, 0, bytes, 0x00020000);
}
__device__ __forceinline__ unsigned pack2(float a, float b) {
  f32x2 v = {a, b};
  return __builtin_bit_cast(unsigned, __builtin_convertvector(v, bf16x2));
}
__device__ __forceinline__ unsigned umin_(unsigned a, unsigned b) { return a < b ? a : b; }
__device__ __forceinline__ unsigned umax_(unsigned a, unsigned b) { return a > b ? a : b; }
__device__ __forceinline__ float bf2f(unsigned x16) { return __uint_as_float(x16 << 16); }
__device__ __forceinline__ float bfhi(unsigned x) { return __uint_as_float(x & 0xffff0000u); }
__device__ __forceinline__ float rbf(float x) { return __uint_as_float(pack2(x, 0.f) << 16); }
__device__ __forceinline__ int fresh_lane() {
  unsigned z;
  asm volatile("s_mov_b32 %0, 0" : "=s"(z));
  const int l = (int)__builtin_amdgcn_mbcnt_hi(~0u, __builtin_amdgcn_mbcnt_lo(~0u, z));
  __builtin_assume(l >= 0 && l < 64);
  return l;
}
__device__ __forceinline__ u32x4 make_sel(int c, int q) {
  u32x4 sel;
  const int e = c - 8 * (q & 1);
  const unsigned one = (e & 1) ? 0x3F800000u : 0x3F80u;
#pragma unroll
  for (int k = 0; k < 4; ++k) sel[k] = ((e >> 1) == k) ? one : 0u;
  asm volatile("s_nop 1" : "+v"(sel));
  return sel;
}
__device__ __forceinline__ int w_src(int lane, int w) {
  const int jp = lane & 3, sf = (lane >> 2) & 7, sq = (lane >> 5) + 2 * (w & 1), ss = w >> 1;
  return (sf * 128 + 32 * sq + 8 * ss + 2 * jp) * 1024;
}
__device__ __forceinline__ void stage_w(char* smem, int lane, int w, u32x4 r0, u32x4 r1) {
  const int jp = lane & 3, sf = (lane >> 2) & 7, sq = (lane >> 5) + 2 * (w & 1), ss = w >> 1;
  const int wdst = L_WB + (ss * 64 + sq * 16 + 2 * sf) * 16 + jp * 4;
#pragma unroll
  for (int h = 0; h < 8; ++h) {
    const unsigned d = __builtin_amdgcn_perm(r1[h >> 1], r0[h >> 1], (h & 1) ? 0x07060302u : 0x05040100u);
    *(unsigned*)(smem + wdst + (h & 3) * 4096 + (h >> 2) * 16) = d;
  }
}
__device__ __forceinline__ int list_off(int lane) {
  const int c = lane & 15, q = lane >> 4;
  return ((q & 1) * 2 + (c >> 3)) * 256 + ((q >> 1) * 8 + (c & 7)) * 16;
}
struct QT { u32x4 t0, t2; };
__device__ __forceinline__ void qt_init(QT& t) { asm volatile("s_nop 7" : "=v"(t.t0), "=v"(t.t2)); }
#define CNDX(perm, o, own, oth, m) do { register u64 m_ asm("vcc") = m; unsigned o_ = o; \
  asm volatile("v_cndmask_b32_dpp %0, %2, %1, vcc quad_perm:" perm " row_mask:0xf bank_mask:0xf" : "+v"(o_) : "v"(own), "v"(oth), "s"(m_)); o = o_; } while (0)
#define CNDXI(perm, own, oth, m) do { register u64 m_ asm("vcc") = m; unsigned o_ = own; \
  asm volatile("v_cndmask_b32_dpp %0, %1, %0, vcc quad_perm:" perm " row_mask:0xf bank_mask:0xf" : "+v"(o_) : "v"(oth), "s"(m_)); own = o_; } while (0)
__device__ __forceinline__ void qt_step(u32x4 (&x)[4], QT& t, int n) {
  const int st = n >> 3, p = (n >> 2) & 1, d = n & 3;
  if (st == 0) {
    if (p == 0) CNDX("[1,0,3,2]", t.t0[d], x[0][d], x[1][d], 0x5555555555555555ull);
    else        CNDX("[1,0,3,2]", t.t2[d], x[2][d], x[3][d], 0x5555555555555555ull);
  } else if (st == 1) {
    if (p == 0) CNDXI("[1,0,3,2]", x[3][d], x[2][d], 0xAAAAAAAAAAAAAAAAull);
    else        CNDXI("[1,0,3,2]", x[1][d], x[0][d], 0xAAAAAAAAAAAAAAAAull);
  } else if (st == 2) {
    if (p == 0) CNDX("[2,3,0,1]", x[0][d], t.t0[d], t.t2[d], 0x3333333333333333ull);
    else        CNDX("[2,3,0,1]", x[2][d], x[1][d], x[3][d], 0x3333333333333333ull);
  } else {
    if (p == 0) CNDXI("[2,3,0,1]", t.t2[d], t.t0[d], 0xCCCCCCCCCCCCCCCCull);
    else        CNDXI("[2,3,0,1]", x[3][d], x[1][d], 0xCCCCCCCCCCCCCCCCull);
  }
  if (n == 31) { const u32x4 dead = x[1]; x[1] = x[2]; x[2] = t.t2; t.t2 = dead; asm volatile("s_nop 1"); SCHED(); }
}
__device__ __forceinline__ void qt_all(u32x4 (&x)[4], QT& t) {
#pragma unroll
  for (int n = 0; n < 32; ++n) qt_step(x, t, n);
}
__device__ __forceinline__ void wait_flags(const char* p, u64 tag) {
  gu64* f = (gu64*)(p + (fresh_lane() & 31) * 8);
#pragma unroll 1
  for (int spins = 0; spins < (1 << 16); ++spins) {
    const u64 v = __hip_atomic_load(f, RLX_AGENT);
    if (__all(v == tag)) break;
    __builtin_amdgcn_s_sleep(2);
  }
  __builtin_amdgcn_fence(__ATOMIC_ACQUIRE, "wavefront");
}
__device__ __forceinline__ float gelu_tanh(float x) {
  const float u = 0.7978845608028654f * (x + 0.044715f * x * x * x);
  const float e = __expf(-2.f * u);
  return x * __builtin_amdgcn_rcpf(1.f + e);
}

__device__ __forceinline__ float row_sum(float x) {
  x += __uint_as_float(__builtin_amdgcn_mov_dpp(__float_as_uint(x), 0xB1, 0xf, 0xf, true));
  x += __uint_as_float(__builtin_amdgcn_mov_dpp(__float_as_uint(x), 0x4E, 0xf, 0xf, true));
  x += __uint_as_float(__builtin_amdgcn_mov_dpp(__float_as_uint(x), 0x141, 0xf, 0xf, true));
  x += __uint_as_float(__builtin_amdgcn_mov_dpp(__float_as_uint(x), 0x140, 0xf, 0xf, true));
  return x;
}

#define E2CAP 18
#define PRIO 1
#define WTD 4
#define E2G 8
#define PREPW 0
#define NB1 6
#define WLK 5
#define P1 5
#ifndef NVI
#define NVI 30
#endif
#define UXLD 6
#define IXD 5
#define TPRE 3
#define TPRE0 1
#define GDEP 4

constexpr int e2cnt(int u) { return u <= 21 ? (E2G + u < E2CAP ? E2G + u : E2CAP) : E2CAP + 2 * (u - 21); }

__global__ void __launch_bounds__(512, 2)
k_fused(const int* __restrict__ idx, const char* __restrict__ table, const u16* __restrict__ wp,
        const char* __restrict__ w1, const u16* __restrict__ b1, const u16* __restrict__ w2,
        const u16* __restrict__ b2, char* __restrict__ ws, u16* __restrict__ out) {
  __shared__ __attribute__((aligned(16))) char smem[L_TOTAL];
  const int w = __builtin_amdgcn_readfirstlane(threadIdx.x >> 6);
  const int bid = blockIdx.x, xl = bid & 7, rr = bid >> 3;
  const int g = xl * 8 + (rr & 7), phi = rr >> 3;
  const rsrc_t rs_w1 = mkrs(w1, NF * ND * NH * 2), rs_tab = mkrs(table, NV * ND * 2), rs_ws = mkrs(ws, WS_END);
  const rsrc_t rs_idx = mkrs(idx, NB * NF * 4);
  const int wsb = phi * 24 * 128 * 1024 + g * 16;
  const int lsb = WS_LIST + (phi * 3 * 256 + w * 32) * 256;
  RSTAMP(62); STAMP(0);

  const u64 tag = *(const __attribute__((address_space(4))) u64*)(ws + WS_EPOCH) * 6364136223846793005ull + 1442695040888963407ull;
  u32x4 T[8][4];
  const int tsb = w * 128 * 256, slice = L_P + w * 16384;
  unsigned dv[4];
  auto dma_t = [&](auto M, auto I) {
    constexpr int m = M.v, i = I.v, slot = m < 4 ? m : m - 3;
    dma16<i * 1024>(dv[i], slice + slot * 4096, rs_tab, tsb + m * 4096);
  };
  u32x4 pi[6];
  unsigned la, lb, wa, wb;
  {
    const int lane = fresh_lane();
    if (w == PREPW) {
      const int c7 = lane & 7, po = c7 * (NF * 4) + (lane >> 4) * 32 + (((lane >> 3) ^ (lane >> 2)) & 1) * 16;
      sfor<0, 6>([&](auto K) { ld16<(K.v & 1) * (8 * NF * 4) + (K.v >> 1) * 128>(pi[K.v], po, rs_idx, bid * 16 * NF * 4); });
      asm volatile("buffer_load_dword %0, %2, %3, %4 offen offset:384\n\tbuffer_load_dword %1, %2, %3, %4 offen offset:3584"
                   : "=&v"(la), "=&v"(lb) : "v"(c7 * (NF * 4) + ((lane >> 3) & 3) * 4), "s"(rs_idx), "s"(bid * 16 * NF * 4));
    }
    u32x4 wp0, wp1;
    asm volatile("buffer_load_dwordx4 %0, %2, %3, %4 offen\n\tbuffer_load_dwordx4 %1, %2, %3, %4 offen offset:1024"
                 : "=&v"(wp0), "=&v"(wp1) : "v"(w_src(lane, w)), "s"(rs_w1), "s"(wsb));
    SCHED();
    if (w == 0 && lane == 0) *(unsigned*)(smem + L_MISC) = 0u;
    {
      const int h = (lane & 15) ^ (lane >> 4);
      dv[0] = (lane >> 4) * 256 + (h ^ ((h & 4) << 1)) * 16;
      dv[1] = dv[0] ^ 192; dv[2] = dv[0] ^ 128; dv[3] = dv[0] ^ 64;
    }
    SCHED();
    asm volatile("s_barrier" ::: "memory");
    if (w == PREPW) {
      const rsrc_t rs_wp = mkrs(wp, NF * NF * 2);
      const int id = bid * 23 + lane, tile = id >> 8, l = (id >> 2) & 63, r = id & 3, c = l & 15, q = l >> 4;
      int tj = (tile >= 1) + (tile >= 3) + (tile >= 6) + (tile >= 10) + (tile >= 15);
      int i = 16 * (tile - tj * (tj + 1) / 2) + 4 * q + r, j = 16 * tj + c;
      bool oka = true, okb = (i >> 4) != tj;
      if (tile >= 21) {
        tj = (tile - 21) * 4 + r; i = 96 + q; j = tj < 6 ? 16 * tj + c : 96 + (c >> 2);
        oka = tj < 6 || (tj == 6 && (c & 3) == 0); okb = tj < 6;
      }
      asm volatile("buffer_load_ushort %0, %2, %4, 0 offen\n\tbuffer_load_ushort %1, %3, %4, 0 offen"
                   : "=&v"(wa), "=&v"(wb) : "v"((oka && lane < 23) ? (i * NF + j) * 2 : 0x100000), "v"((okb && lane < 23) ? (j * NF + i) * 2 : 0x100000), "s"(rs_wp));
      asm volatile("s_waitcnt vmcnt(4)" : "+v"(la), "+v"(lb), "+v"(pi[0]), "+v"(pi[1]), "+v"(pi[2]), "+v"(pi[3]), "+v"(pi[4]), "+v"(pi[5]));
      const int jb = (((lane >> 3) ^ (lane >> 2)) & 1) * 4;
      const unsigned k0 = jb * 0x10001u + 0x10000u, k1 = k0 + 0x20002u, sh = (lane & 1) * 16;
      const bool sw = lane & 2;
#pragma unroll
      for (int k = 0; k < 3; ++k) {
        u32x4 o;
#pragma unroll
        for (int tp = 0; tp < 2; ++tp) {
          const u32x4 a = pi[2 * k + tp];
          const unsigned p0 = ((a[0] | (a[1] << 16)) << 3) + k0, p1 = ((a[2] | (a[3] << 16)) << 3) + k1;
          const unsigned r0 = __builtin_amdgcn_alignbit(p0, p0, sh), r1 = __builtin_amdgcn_alignbit(p1, p1, sh);
          o[2 * tp] = sw ? r1 : r0; o[2 * tp + 1] = sw ? r0 : r1;
        }
        BST128C(o, rs_ws, (lane >> 4) * 65536 + (lane & 15) * 16, WS_LIST + k * 4 * 65536 + bid * 256);
      }
      __builtin_amdgcn_raw_buffer_store_b32(la | (lb << 16), rs_ws, lane < 32 ? (lane >> 3) * 8192 + (lane & 7) * 16 : 0x7f000000,
                                            WS_LX + (bid >> 5) * 1024 + (bid & 7) * 128 + ((bid >> 3) & 3) * 4, SC1);
    }
    sfor<0, 4>([&](auto I) { dma_t(IC<0>{}, I); });
    if (w == PREPW) asm volatile("s_waitcnt vmcnt(10)" ::: "memory"); else asm volatile("s_waitcnt vmcnt(4)" ::: "memory");
    asm volatile("" : "+v"(wp0), "+v"(wp1) :: "memory");
    stage_w(smem, lane, w, wp0, wp1);
    if (w == PREPW) {
      asm volatile("s_waitcnt vmcnt(8)" : "+v"(wa), "+v"(wb));
      __builtin_amdgcn_raw_buffer_store_b32(__float_as_uint(bf2f(wa) + bf2f(wb)), rs_ws, lane < 23 ? lane * 4 : 0x7f000000, WS_WT + bid * 92, SC1);
    }
    SCHED();
    sfor<0, 4>([&](auto I) { dma_t(IC<1>{}, I); });
    sfor<0, 4>([&](auto I) { dma_t(IC<2>{}, I); });
  }
  STAMP(1);
  BAR();
  if (w == PREPW) {
    DRAIN();
    if (fresh_lane() == 0) __hip_atomic_store((gu64*)(ws + WS_F1 + bid * 8), tag, RLX_AGENT);
  }
  STAMP(2);
  if (P1 && w >= 4) __builtin_amdgcn_s_setprio(1);
  auto touch_l = [&](int ph) -> unsigned {
    const int lane = fresh_lane();
    return __builtin_amdgcn_raw_buffer_load_b32(rs_ws, lane < 8 ? ((rr & 7) * 8 + lane) * 128 : 0x7f000000, lsb + ph * 65536, SC1);
  };

  u32x4 ix[IXD];
  auto load_ix = [&](int ph, int k0, int k1) {
    const int lsrc = list_off(fresh_lane());
#pragma unroll
    for (int k = k0; k < k1; ++k) ix[k] = BLD128C(rs_ws, lsrc + (k & 3) * 1024, lsb + ph * 65536 + (k >> 2) * 4096);
  };
  f32x4 acc[16];
  {
    const int lane = fresh_lane(), c = lane & 15, q = lane >> 4;
    int pw = L_P + (128 * w + 4 * q) * 128 + c * 8;
    u32x4 Wf[4][4];
#pragma unroll
    for (int t = 0; t < 4; ++t)
#pragma unroll
      for (int s = 0; s < 4; ++s) Wf[t][s] = *(const u32x4*)(smem + L_WB + (t * 4 + s) * 1024 + lane * 16);
    SCHED();
    const int toff = ((lane >> 2) & 3) * 1024 + (lane >> 4) * 64 + (lane & 3) * 16;
    int ra[4];
    ra[0] = slice + c * 256 + (((q ^ ((q & 1) << 1)) << 2) ^ c) * 16;
    ra[1] = ra[0] ^ 16; ra[2] = ra[0] ^ 32; ra[3] = ra[0] ^ 48;
    asm volatile("" : "+v"(ra[0]), "+v"(ra[1]), "+v"(ra[2]), "+v"(ra[3]), "+v"(pw));
    auto read_t = [&](int m, int s) { T[m][s] = *(const u32x4*)(smem + ra[s] + (m < 4 ? m : m - 3) * 4096); };
    auto load_t = [&](auto M, auto I) {
      ld16<I.v * 256>(T[M.v][I.v], toff, rs_tab, tsb + M.v * 4096);
    };
    asm volatile("s_waitcnt vmcnt(8)" ::: "memory");
#pragma unroll
    for (int s = 0; s < 4; ++s) read_t(0, s);
    u64 fl;
    u32x2 bz;
    u32x4 xr0, xr1;
    QT qt; qt_init(qt);
    f32x4 D[2][2];
    RESERVE(D[0][0]); RESERVE(D[0][1]); RESERVE(D[1][0]); RESERVE(D[1][1]);
    u32x2 o[4];
    SCHED();
    auto block = [&](auto M) {
      constexpr int m = M.v;
      if constexpr (m == P1) __builtin_amdgcn_s_setprio(0);
      sfor<0, 16>([&](auto K) {
        constexpr int k = K.v, h = k >> 3, s = (k >> 1) & 3, t = k & 1;
        if constexpr (m == 8 && k == 0) asm volatile("s_nop 7");
        if constexpr ((m == 3 || m == 6) && k == 0)
          wait4<(m == 3 ? 4 : 0)>(T[m + 1]);
        if constexpr (m < 8) mf<s == 0>(D[h][t], T[m][s], Wf[2 * h + t][s]);
        SCHED();
        if constexpr (m == 3 || m == 6) { qt_step(T[m + 1], qt, 2 * k); qt_step(T[m + 1], qt, 2 * k + 1); }
        if constexpr ((k & 3) == 1 && m < 5) {
          if constexpr (m == 1 || m == 4) load_t(IC<m + 3>{}, IC<(k >> 2)>{}); else dma_t(IC<m + 3>{}, IC<(k >> 2)>{});
        }
        if constexpr (m != 3 && m < 6 && k >= 8 && k < 12) {
          if constexpr (k == 8) asm volatile("s_waitcnt vmcnt(%0)" :: "i"(m < 5 ? 6 : 4) : "memory");
          read_t(m + 1, k - 8);
        }
        if constexpr ((m == 1 || m == 2) && k == 15) asm volatile("s_waitcnt lgkmcnt(0)" ::: "memory");
        if constexpr (m == 6 && k == 1) {
          fl = __hip_atomic_load((gu64*)(ws + WS_F1 + w * 256 + (fresh_lane() & 31) * 8), RLX_AGENT);
          bz = __builtin_amdgcn_raw_buffer_load_b64(mkrs(b1, phi == 0 ? NH * 2 : 0), ((fresh_lane() >> 4) & 1) * 8, g * 16, 0);
          if (w == 7) {
            const int l2 = fresh_lane();
            xr0 = BLD128(rs_w1, l2 * 2048, (96 + phi) * 128 * 1024 + g * 16);
            xr1 = BLD128(rs_w1, l2 * 2048 + 1024, (96 + phi) * 128 * 1024 + g * 16);
          }
        }
        if constexpr (m == 7 && k == 0) {
          if (!__all(fl == tag)) wait_flags(ws + WS_F1 + w * 256, tag);
          __builtin_amdgcn_fence(__ATOMIC_ACQUIRE, "wavefront");
          load_ix(0, 0, IXD);
        }
        if constexpr (m > 0 && k >= 4 && k < 8) {
          constexpr int r = k - 4;
          o[r][1] = pack2(D[1][0][r], D[1][1][r]);
          *(u32x2*)(smem + pw + (m - 1) * 2048 + r * 128) = o[r];
        }
        if constexpr (m < 8 && k >= 12) o[k - 12][0] = pack2(D[0][0][k - 12], D[0][1][k - 12]);
        SCHED();
      });
    };
    sfor<0, 8>(block);
    if (w < 7) block(IC<8>{});
    else {
      const int l2 = fresh_lane(), xd = L_WX + (((l2 >> 2) & 3) * 4 + (l2 >> 4)) * 128 + (l2 & 3) * 4;
#pragma unroll
      for (int h = 0; h < 8; ++h) {
        const unsigned d = __builtin_amdgcn_perm(xr1[h >> 1], xr0[h >> 1], (h & 1) ? 0x07060302u : 0x05040100u);
        *(unsigned*)(smem + xd + h * 16) = d;
      }
    }
    const f32x4 a0 = {bf2f(bz[0]), bfhi(bz[0]), bf2f(bz[1]), bfhi(bz[1])};
#pragma unroll
    for (int i = 0; i < 16; ++i) acc[i] = a0;
  }
  STAMP(3);
  BAR();
  STAMP(4);

  auto blank = [&](int k0, int k1) {
    u32x4 ones = {~0u, ~0u, ~0u, ~0u};
    opaque(ones);
    const int l16 = fresh_lane() * 16;
#pragma unroll
    for (int k = k0; k < k1; ++k) BST128C(ones, rs_ws, l16 + (k & 3) * 1024, WS_X + bid * 65536 + w * 16384 + (k >> 2) * 4096);
  };
  sfor<0, 2>([&](auto PH) {
    constexpr int ph = PH.v;
    sfor<0, 8>([&](auto M) { pin4<M.v>(T[M.v]); });
    u32x4 lx;
    {
      const int lane = fresh_lane(), c = lane & 15, q = lane >> 4;
      const int lsrc = list_off(lane);
      u32x4 wr0, wr1;
      auto load_w = [&](int v) {
        const int wsrc = w_src(fresh_lane(), v);
        wr0 = BLD128(rs_w1, wsrc, wsb + (ph + 1) * 8 * 128 * 1024); wr1 = BLD128(rs_w1, wsrc + 1024, wsb + (ph + 1) * 8 * 128 * 1024);
      };
      const u32x4 sel = make_sel(c, q);
      constexpr int DEP = GDEP;
      u32x4 gv[DEP];
      auto gaddr = [&](int n) -> unsigned {
        const unsigned x = ix[(n >> 3) % IXD][(n >> 1) & 3];
        return ((n & 1) ? (x >> 16) : (x & 0xffffu)) << 4;
      };
      u32x4 Wx[4];
      f32x4 dx[2];
      int xw = L_PX + (128 * w + c) * 16 + (q & 1) * 8;
      if constexpr (ph == 0) {
#pragma unroll
        for (int s = 0; s < 4; ++s) Wx[s] = *(const u32x4*)(smem + L_WX + (s * 4 + q) * 128 + (c & 7) * 16);
        RESERVE(dx[0]); RESERVE(dx[1]);
        asm volatile("" : "+v"(xw));
      }
#pragma unroll
      for (int n = 0; n < DEP; ++n) gv[n] = *(const u32x4*)(smem + L_P + gaddr(n));
      sfor<0, 64>([&](auto N) {
        constexpr int n = N.v;
        if constexpr (ph == 0 && n < 32) mf<(n & 3) == 0>(dx[(n >> 2) & 1], Wx[n & 3], T[n >> 2][n & 3]);
        if constexpr (ph == 0 && n >= 4 && n < 36 && (n & 3) == 3) {
          constexpr int m = (n >> 2) - 1;
          SCHED();
          if (m < 7 || w < 7) *(u32x2*)(smem + xw + m * 256) = (u32x2){pack2(dx[m & 1][0], dx[m & 1][1]), pack2(dx[m & 1][2], dx[m & 1][3])};
          SCHED();
        }
        if constexpr (ph == 0 && (n & 3) == 0) acc[n >> 2] = MFMA(sel, gv[n % DEP], acc[n >> 2]); else mfma_acc<(n >> 2)>(acc[n >> 2], sel, gv[n % DEP]);
        SCHED();
        if (n + DEP < 64) gv[n % DEP] = *(const u32x4*)(smem + L_P + gaddr(n + DEP));
        if (((n + DEP) & 7) == 7) {
          const int k = ((n + DEP) >> 3) + IXD;
          if (k < 8) ix[k % IXD] = BLD128C(rs_ws, lsrc + (k & 3) * 1024, lsb + ph * 65536 + (k >> 2) * 4096);
          if (k == WLK && w < 4) load_w(w);
          if (k == WLK + 3 && w < 4) { stage_w(smem, fresh_lane(), w, wr0, wr1); load_w(w + 4); }
          if (k == 10 && ph == 0 && w >= 4) lx = BLD128C(rs_ws, fresh_lane() * 16, WS_LX + phi * 8192 + w * 1024);
        }
        SCHED();
      });
      if (w < 4) {
        const int l2 = fresh_lane();
        u32x4 wt[4];
        if constexpr (ph == 1) {
#pragma unroll
          for (int k = 0; k < 4; ++k) wt[k] = BLD128C(rs_ws, l2 * 16 + k * 1024, WS_WT + w * 4096);
        }
        stage_w(smem, l2, w + 4, wr0, wr1);
        if constexpr (ph == 0) blank(0, NB1);
        else {
#pragma unroll
          for (int k = 0; k < 4; ++k) if (k < 3 || w < 3) *(u32x4*)(smem + L_WT + w * 4096 + k * 1024 + l2 * 16) = wt[k];
          DRAIN();
        }
      }
    }
    STAMPL(5);
    BAR();
    if (ph == 1 && w == 0 && fresh_lane() == 0) __hip_atomic_store((gu64*)(ws + WS_F2 + bid * 8), tag, RLX_AGENT);
    STAMPL(6);
    auto extra_gather = [&]() {
      const int lane = fresh_lane(), c = lane & 15, q = lane >> 4;
      u32x4 gv[8];
#pragma unroll
      for (int n = 0; n < 8; ++n) gv[n] = *(const u32x4*)(smem + L_PX + (((n & 1) ? (lx[n >> 1] >> 16) : (lx[n >> 1] & 0xffffu)) << 4));
      SCHED();
      const u32x4 sel = make_sel(c, q);
      u32x4 slo = q < 2 ? sel : (u32x4){0, 0, 0, 0}, shi = q < 2 ? (u32x4){0, 0, 0, 0} : sel;
      asm volatile("s_nop 1" : "+v"(slo), "+v"(shi));
      SCHED();
      sfor<0, 8>([&](auto N) {
        constexpr int n = N.v, a = 2 * (n & 6) + (n & 1);
        mfma_acc<a>(acc[a], slo, gv[n]);
        mfma_acc<a + 2>(acc[a + 2], shi, gv[n]);
      });
    };
    if constexpr (ph == 0) { if (w >= 4) extra_gather(); }
    SCHED();
    sfor<0, 8>([&](auto M) { pin4<M.v>(T[M.v]); });
    {
      const int lane = fresh_lane(), c = lane & 15, q = lane >> 4;
      int pw = L_P + (128 * w + 4 * q) * 128 + c * 8, wb = L_WB + lane * 16, eo;
      asm volatile("" : "+v"(pw), "+v"(wb));
      u32x4 Wf[2][4];
#pragma unroll
      for (int t = 0; t < 2; ++t)
#pragma unroll
        for (int s = 0; s < 4; ++s) Wf[t][s] = *(const u32x4*)(smem + wb + (t * 4 + s) * 1024);
      f32x4 D[2][2];
      RESERVE(D[0][0]); RESERVE(D[0][1]); RESERVE(D[1][0]); RESERVE(D[1][1]);
      unsigned tl, pk[4];
      auto um = [](int u) { return ph ? (u >> 2) * 2 + (u & 1) : (u & 7); };
      auto uh = [](int u) { return ph ? ((u >> 1) ^ (u >> 2)) & 1 : (u >> 3); };
      sfor<0, 18>([&](auto U) {
        constexpr int u = U.v, m = um(u), h = uh(u);
        sfor<0, 8>([&](auto J) {
          constexpr int j = J.v, s = j >> 1, t = j & 1;
          if constexpr (u < 16) {
            mf<s == 0>(D[u & 1][t], T[m][s], Wf[t][s]);
            SCHED();
            if constexpr (u < 15 && uh(u + 1) != h) Wf[t][s] = *(const u32x4*)(smem + wb + (((h ^ 1) * 2 + t) * 4 + s) * 1024);
          }
          if constexpr (u >= 2 && j < 2) {
            *(unsigned*)(smem + pw + j * 256) = pk[2 * j];
            *(unsigned*)(smem + pw + j * 256 + 128) = pk[2 * j + 1];
          }
          if constexpr (u >= 2 && u < 17 && j == 2) { pw += (um(u - 1) - um(u - 2)) * 2048 + (uh(u - 1) - uh(u - 2)) * 4; asm volatile("" : "+v"(pw)); }
          if constexpr (u == 1 && j == 3) asm volatile("s_nop 2");
          if constexpr (u == 16 && j == 3) asm volatile("s_nop 3");
          if constexpr (u >= 1 && u < 17 && j >= 4) pk[j - 4] = pack2(D[(u - 1) & 1][0][j - 4], D[(u - 1) & 1][1][j - 4]);
          if constexpr (u == 2 && j == 3) tl = touch_l(ph + 1);
          if constexpr (ph == 1) {
            if constexpr (u == 4 && j < 7) {
              if constexpr (j == 0) { eo = ((wb - L_WB) >> 2) & 48; asm volatile("" : "+v"(eo)); }
              if constexpr (j < 6) T[j >> 2][j & 3] = BLD128(rs_idx, eo + j * 64, (bid * 16 + w * 2) * NF * 4);
              else {
                T[1][2][0] = __builtin_amdgcn_raw_buffer_load_b32(rs_idx, (eo >> 2) + 384, (bid * 16 + w * 2) * NF * 4, 0);
                eo = (((wb - L_WB) >> 2) & 192) + ((wb - L_WB) & 48); asm volatile("" : "+v"(eo));
              }
            }
            if constexpr ((u == 7 || u == 8 || u == 11 || u == 12 || u == 15 || u == 16) && (j & 1) == 1) {
              constexpr int b = (u - 6) >> 1;
              T[b + 2][s] = BLD128(rs_tab, T[b >> 2][b & 3][s] * 256 + eo, 0);
            }
            if constexpr (u == 17 && j == 0) T[1][3] = BLD128(rs_tab, T[1][2][0] * 256 + eo, 0);
            if constexpr (u == 10 && j == 3) T[0][0] = BLD128C(rs_ws, wb - L_WB, WS_WT + (15 + w) * 1024);
            if constexpr (u == 14 && j == 3) {
              const u32x2 f = __builtin_amdgcn_raw_buffer_load_b64(rs_ws, ((wb - L_WB) >> 1) & 255, WS_F2 + w * 256, SC1);
              T[0][3][0] = f[0]; T[0][3][1] = f[1];
            }
          }
          if constexpr (u == 12 && j == 3) { asm volatile("" :: "v"(tl)); load_ix(ph + 1, 0, 1); }
          if constexpr (u == 15 && j == 7) {
            if constexpr (ph == 0) { if (w < 4) lx = BLD128C(rs_ws, wb - L_WB, WS_LX + phi * 8192 + w * 1024); }
            load_ix(ph + 1, 1, IXD);
          }
          SCHED();
        });
      });
    }
    if constexpr (ph == 0) { if (w < 4) { blank(NB1, 16); extra_gather(); } }
    STAMPL(7);
    BAR();
    STAMPL(8);
  });

  if (PRIO && w >= 4) __builtin_amdgcn_s_setprio(1);
  u32x4 E[7][4], E2[7][4], vi[7];
  int eoff;
  auto load_e2 = [&](auto J) {
    constexpr int k = J.v;
    if constexpr (k < 24 && (k & 3) == 0) pin1<16 + (k >> 2)>(vi[k >> 2]);
    if constexpr (k < 24) E2[k >> 2][k & 3] = BLD128(rs_tab, vi[k >> 2][k & 3] * 256 + eoff, 0);
  };
  QT qt;
  {
    const int lane = fresh_lane(), c = lane & 15, q = lane >> 4;
    const int lsrc = list_off(lane);
    u32x4 jx[8];
#pragma unroll
    for (int t = 0; t < 6; ++t)
#pragma unroll
      for (int i = 0; i < 4; ++i) E[t][i] = T[t + 2][i];
    E[6][0] = T[1][3];
#pragma unroll
    for (int k = 0; k < IXD; ++k) jx[k] = ix[k];
#pragma unroll
    for (int k = IXD; k < 8; ++k) jx[k] = BLD128C(rs_ws, lsrc + (k & 3) * 1024, lsb + 2 * 65536 + (k >> 2) * 4096);
    const u32x4 sel = make_sel(c, q);
    const int xo = ((q >> 1) * 2 + (c >> 3)) * 65536 + ((q & 1) * 8 + (c & 7)) * 16;
    const int xb = WS_X + (w * 32 * 256 + g * 4 + phi) * 256;
    constexpr int DEP = GDEP;
    u32x4 gv[DEP];
    auto gaddr = [&](int n) -> unsigned {
      const unsigned x = jx[n >> 3][(n >> 1) & 3];
      return ((n & 1) ? (x >> 16) : (x & 0xffffu)) << 4;
    };
#pragma unroll
    for (int n = 0; n < DEP; ++n) gv[n] = *(const u32x4*)(smem + L_P + gaddr(n));
    *(u32x4*)(smem + L_WT + (15 + w) * 1024 + lane * 16) = T[0][0];
    asm volatile("" ::: "memory");
    if (lane == 0) __hip_atomic_fetch_add((unsigned*)(smem + L_MISC), 1u, __ATOMIC_RELAXED, __HIP_MEMORY_SCOPE_WORKGROUP);
    if (!__all((((u64)T[0][3][1] << 32) | T[0][3][0]) == tag)) wait_flags(ws + WS_F2 + w * 256, tag);
    qt_init(qt);
    SCHED();
    sfor<0, 68>([&](auto N) {
      constexpr int n = N.v;
      if constexpr (n < 64) mfma_acc<(n >> 2)>(acc[n >> 2], sel, gv[n % DEP]);
      SCHED();
      if constexpr (n + DEP < 64) gv[n % DEP] = *(const u32x4*)(smem + L_P + gaddr(n + DEP));
      if constexpr (n == 64) asm volatile("s_nop 3" : "+{v[92:95]}"(E[6][0]));
      if constexpr (n >= 64 - 2 * E2G && n < 64 && (n & 1) == 0) load_e2(IC<(n - 64 + 2 * E2G) / 2>{});
      if constexpr (n == NVI) {
        eoff = (lane >> 4) * 64 + (lane & 3) * 16;
#pragma unroll
        for (int t = 0; t < 6; ++t) vi[t] = BLD128(rs_idx, ((lane >> 2) & 3) * 16 + t * 64, (bid * 16 + w * 2 + 1) * NF * 4);
      }
      if constexpr (n >= 11 && (n & 7) == 3) {
        constexpr int ch = (n >> 3) - 1;
        const f32x4 a = acc[2 * ch], b = acc[2 * ch + 1];
        BST128C(((u32x4){umin_(pack2(a[0], a[1]), ~1u), umin_(pack2(a[2], a[3]), ~1u), umin_(pack2(b[0], b[1]), ~1u), umin_(pack2(b[2], b[3]), ~1u)}), rs_ws, xo, xb + ch * 4 * 65536);
      }
      if constexpr (n < 64) sfor<3 * n, 3 * n + 3>([&](auto K) { if constexpr ((K.v & 31) == 0) pin4<(K.v >> 5) + 2>(E[K.v >> 5]); qt_step(E[K.v >> 5], qt, K.v & 31); });
      SCHED();
    });
  }
  if (PRIO == 2) __builtin_amdgcn_s_setprio(0);
  STAMP(13);

#pragma unroll 1
  for (int spins = 0; spins < (1 << 20); ++spins) {
    if (__builtin_amdgcn_readfirstlane(__hip_atomic_load((unsigned*)(smem + L_MISC), __ATOMIC_RELAXED, __HIP_MEMORY_SCOPE_WORKGROUP)) == 8u) break;
    __builtin_amdgcn_s_sleep(1);
  }
  asm volatile("" ::: "memory");
  STAMP(14);
  u32x4 X[8];
  u32x2 ww[4];
  auto pnn = [&](u32x4 (&Ee)[7][4], u32x4 (&En)[7][4], auto nxt, auto hook) -> float {
    int wa = L_WT + fresh_lane() * 16;
    asm volatile("" : "+v"(wa));
    f32x4 sm = ZERO4, D[3], wt[WTD];
    RESERVE(D[0]); RESERVE(D[1]); RESERVE(D[2]);
    sfor<0, WTD - 2>([&](auto J) { wt[J.v] = *(const f32x4*)(smem + wa + J.v * 1024); });
    sfor<0, 30>([&](auto U) {
      constexpr int u = U.v;
      constexpr int tj = u < 1 ? 0 : u < 3 ? 1 : u < 6 ? 2 : u < 10 ? 3 : u < 15 ? 4 : u < 21 ? 5 : 6, ti = u - tj * (tj + 1) / 2;
      if constexpr (u >= 1 && u + WTD - 3 < 23) wt[(u + WTD - 3) % WTD] = *(const f32x4*)(smem + wa + (u + WTD - 3) * 1024);
      sfor<0, 4>([&](auto S) {
        constexpr int s = S.v, k = u * 4 + s;
        if constexpr (u < 21) mf<s == 0>(D[u % 3], Ee[ti][s], Ee[tj][s]);
        else if constexpr (u < 28) mf<s == 0>(D[u % 3], Ee[6][s], Ee[ti][s]);
        else if constexpr (s == 0) asm volatile("s_nop 3");
        SCHED();
        constexpr int b = k < 12 ? 2 : k < 24 ? 3 : k < 40 ? 4 : k < 60 ? 5 : 6, k0 = k < 12 ? 0 : k < 24 ? 12 : k < 40 ? 24 : 40, nm = k < 24 ? 12 : k < 40 ? 16 : 20;
        if constexpr (!nxt.v && b < 6) sfor<((k - k0) * 32) / nm, ((k - k0 + 1) * 32) / nm>([&](auto N) { if constexpr (N.v == 0) pin4<8 + b>(Ee[b]); qt_step(Ee[b], qt, N.v); });
        if constexpr (k >= 64 && k < 80) {
          constexpr int j = 3 - ((k - 64) >> 2), d = k & 3;
          Ee[6][j][d] = __builtin_amdgcn_mov_dpp(Ee[6][0][d], j * 0x55, 0xf, 0xf, true);
        }
        if constexpr (nxt.v && k >= 80 && k < 112) { if constexpr ((k & 15) == 0) pin4<8 + ((k - 80) >> 4)>(En[(k - 80) >> 4]); qt_step(En[(k - 80) >> 4], qt, (2 * k) & 31); qt_step(En[(k - 80) >> 4], qt, (2 * k + 1) & 31); }
        if constexpr (u >= 2 && u - 2 < 21) { sm[s] += D[(u - 2) % 3][s] * wt[(u - 2) % WTD][s]; asm volatile("" : "+v"(sm[s])); }
        if constexpr (u - 2 >= 21 && s == 3) { sm[0] += D[(u - 2) % 3][0] * wt[(21 + ((u - 2 - 21) >> 2)) % WTD][(u - 2 - 21) & 3]; asm volatile("" : "+v"(sm[0])); }
        SCHED();
      });
      hook(U);
    });
    return row_sum((sm[0] + sm[1]) + (sm[2] + sm[3]));
  };
  auto load_x = [&]() {
    const int lane = fresh_lane();
#pragma unroll
    for (int k = 0; k < 8; ++k) X[k] = BLD128C(rs_ws, lane * 16 + (k & 3) * 1024, WS_X + (bid * 256 + w * 32) * 256 + (k >> 2) * 4096);
  };
  const float lp0 = pnn(E, E2, IC<1>{}, [&](auto U) {
    if constexpr (U.v == 21) vi[6][0] = __builtin_amdgcn_raw_buffer_load_b32(rs_idx, (fresh_lane() & 12) + 384, (bid * 16 + w * 2 + 1) * NF * 4, 0);
    sfor<e2cnt(U.v), e2cnt(U.v + 1)>(load_e2);
    if constexpr (U.v == 29) E2[6][0] = BLD128(rs_tab, vi[6][0] * 256 + eoff, 0);
    SCHED();
  });
  if (PRIO == 1) __builtin_amdgcn_s_setprio(0);
  STAMP(15);
  const float lp1 = pnn(E2, E2, IC<0>{}, [&](auto U) {
    if constexpr (U.v == UXLD) {
      const int lane = fresh_lane();
#pragma unroll
      for (int j = 0; j < 4; ++j) ww[j] = *(const u32x2*)(w2 + (8 * w + 2 * j + (lane >> 5)) * 8 + ((lane >> 3) & 1) * 4);
      load_x();
      SCHED();
    }
  });
#pragma unroll 1
  for (int spins = 0; spins < (1 << 14); ++spins) {
    unsigned mx = 0;
#pragma unroll
    for (int k = 0; k < 8; ++k) mx = umax_(umax_(umax_(mx, X[k][0]), X[k][1]), umax_(X[k][2], X[k][3]));
    if (!__any(mx == ~0u)) break;
    __builtin_amdgcn_s_sleep(4);
    asm volatile("" ::: "memory");
    load_x();
  }
  STAMP(16);

  {
    const int lane = fresh_lane(), c = lane & 15;
    const u32x4 slo = make_sel(c, 0), shi = make_sel(c, 1);
    float s0 = 0.f;
#pragma unroll
    for (int j = 0; j < 4; ++j) {
      const f32x4 z = MFMA(shi, X[2 * j + 1], MFMA(slo, X[2 * j], ZERO4));
#pragma unroll
      for (int r = 0; r < 4; ++r) s0 += gelu_tanh(z[r]) * ((r & 1) ? bfhi(ww[j][r >> 1]) : bf2f(ww[j][r >> 1]));
    }
    s0 += __uint_as_float(__builtin_amdgcn_mov_dpp(__float_as_uint(s0), 0x128, 0xf, 0xf, true));
    float* red = (float*)(smem + L_RED);
    if (c < 8) red[(((lane >> 4) & 1) * 8 + c) * 20 + 2 * w + (lane >> 5)] = s0;
    if (c == 0) { red[(2 * w) * 20 + 16 + (lane >> 4)] = lp0; red[(2 * w + 1) * 20 + 16 + (lane >> 4)] = lp1; }
    STAMP(17);
    BAR();
    if (w == 0 && lane < 16) {
      const f32x4* rp = (const f32x4*)(red + lane * 20);
      const f32x4 a = (rp[0] + rp[1]) + (rp[2] + rp[3]), b = rp[4];
      const float ld = rbf(rbf((a[0] + a[1]) + (a[2] + a[3])) + bf2f(b2[0]));
      out[bid * 16 + lane] = (u16)(pack2(rbf((b[0] + b[1]) + (b[2] + b[3])) + ld, 0.f) & 0xffffu);
      if (bid == 0 && lane == 0) __hip_atomic_store((gu64*)(ws + WS_EPOCH), tag, RLX_AGENT);
    }
  }
  STAMP(18);
  RSTAMP(63);
}

extern "C" void kernel_launch(void* const* d_in, const int* in_sizes, int n_in, void* d_out, int out_size,
                              void* d_ws, size_t ws_size, hipStream_t stream) {
  k_fused<<<256, 512, 0, stream>>>((const int*)d_in[0], (const char*)d_in[1], (const u16*)d_in[2], (const char*)d_in[3],
                                   (const u16*)d_in[4], (const u16*)d_in[5], (const u16*)d_in[6], (char*)d_ws, (u16*)d_out);
}
```

```cpp
#include <hip/hip_runtime.h>
#include <hip/hip_bf16.h>
#include <stdint.h>

typedef short bf16x8 __attribute__((ext_vector_type(8)));
typedef float f32x4 __attribute__((ext_vector_type(4)));
typedef float f32x2 __attribute__((ext_vector_type(2)));
typedef __bf16 bf16x2 __attribute__((ext_vector_type(2)));
typedef unsigned u32x4 __attribute__((ext_vector_type(4)));
typedef unsigned u32x2 __attribute__((ext_vector_type(2)));
typedef unsigned short u16;
typedef unsigned long long u64;
typedef __attribute__((address_space(1))) u64 gu64;
typedef __amdgpu_buffer_rsrc_t rsrc_t;

#define NSTAMP 19
constexpr int NB = 4096, NF = 100, ND = 128, NV = 1000, NH = 512;

constexpr int WS_EPOCH = 0;
constexpr int WS_F1   = 256;
constexpr int WS_F2   = 4096;
constexpr int WS_LX   = 32768;
constexpr int WS_WT   = 65536;
constexpr int WS_LIST = 131072;
constexpr int WS_X    = 1 << 20;
constexpr int WS_END  = WS_X + (16 << 20);
constexpr size_t WS_STAMP = 32u << 20;

#define STAMP(k) do { if constexpr ((k) == 2) { SCHED(); tk = __builtin_amdgcn_s_memrealtime(); SCHED(); } } while (0)
#define STAMPL(k) STAMP(k + 4 * ph)
#define RSTAMP(k)

constexpr int L_P  = 0;
constexpr int L_WX = 1008 * 128;
constexpr int L_PX = L_WX + 2048;
constexpr int L_WB = L_PX + 1008 * 16;
constexpr int L_MISC = L_WB + 16384;
constexpr int L_TOTAL = L_MISC + 256;
constexpr int L_WT = L_PX;
constexpr int L_WT6 = L_WT + 21 * 1024;
constexpr int L_RED = L_WX;
static_assert(L_TOTAL == 163840, "lds");

#define MFMA(a, b, c) __builtin_amdgcn_mfma_f32_16x16x32_bf16(__builtin_bit_cast(bf16x8, a), __builtin_bit_cast(bf16x8, b), c, 0, 0, 0)
template <bool FIRST> __device__ __forceinline__ void mf(f32x4& d, const u32x4& a, const u32x4& b) {
  if constexpr (FIRST) asm volatile("v_mfma_f32_16x16x32_bf16 %0, %1, %2, 0" : "+v"(d) : "v"(a), "v"(b));
  else asm volatile("v_mfma_f32_16x16x32_bf16 %0, %1, %2, %0" : "+v"(d) : "v"(a), "v"(b));
}
template <int R> __device__ __forceinline__ void pin1(u32x4& x) {
  if constexpr (R == 16) asm volatile("" : "+{v[64:67]}"(x));
  if constexpr (R == 17) asm volatile("" : "+{v[68:71]}"(x));
  if constexpr (R == 18) asm volatile("" : "+{v[72:75]}"(x));
  if constexpr (R == 19) asm volatile("" : "+{v[76:79]}"(x));
  if constexpr (R == 20) asm volatile("" : "+{v[80:83]}"(x));
  if constexpr (R == 22) asm volatile("" : "+{v[88:91]}"(x));
  if constexpr (R == 21) asm volatile("" : "+{v[84:87]}"(x));
}
template <int OFF> __device__ __forceinline__ void dma16(unsigned vo, int lds, rsrc_t rs, int so) {
  asm volatile("s_mov_b32 m0, %1\n\ts_nop 0\n\tbuffer_load_dwordx4 %0, %2, %3 offen offset:%4 lds" :: "v"(vo), "s"(lds), "s"(rs), "s"(so), "i"(OFF) : "memory");
}
template <int OFF> __device__ __forceinline__ void ld16(u32x4& x, unsigned vo, rsrc_t rs, int so) {
  asm volatile("buffer_load_dwordx4 %0, %1, %2, %3 offen offset:%4" : "=v"(x) : "v"(vo), "s"(rs), "s"(so), "i"(OFF));
}
template <int N> __device__ __forceinline__ void wait4(u32x4 (&x)[4]) {
  asm volatile("s_waitcnt vmcnt(%4)" : "+v"(x[0]), "+v"(x[1]), "+v"(x[2]), "+v"(x[3]) : "i"(N));
}
template <class X> __device__ __forceinline__ void opaque(X& x) { asm volatile("" : "+v"(x)); }
#define RESERVE(x) asm volatile("" : "=v"(x))
template <int I> __device__ __forceinline__ void mfma_acc(f32x4& c, const u32x4& a, const u32x4& b) {
  if constexpr (I == 0) asm volatile("v_mfma_f32_16x16x32_bf16 %0, %1, %2, %0" : "+{v[192:195]}"(c) : "v"(a), "v"(b));
  if constexpr (I == 1) asm volatile("v_mfma_f32_16x16x32_bf16 %0, %1, %2, %0" : "+{v[196:199]}"(c) : "v"(a), "v"(b));
  if constexpr (I == 2) asm volatile("v_mfma_f32_16x16x32_bf16 %0, %1, %2, %0" : "+{v[200:203]}"(c) : "v"(a), "v"(b));
  if constexpr (I == 3) asm volatile("v_mfma_f32_16x16x32_bf16 %0, %1, %2, %0" : "+{v[204:207]}"(c) : "v"(a), "v"(b));
  if constexpr (I == 4) asm volatile("v_mfma_f32_16x16x32_bf16 %0, %1, %2, %0" : "+{v[208:211]}"(c) : "v"(a), "v"(b));
  if constexpr (I == 5) asm volatile("v_mfma_f32_16x16x32_bf16 %0, %1, %2, %0" : "+{v[212:215]}"(c) : "v"(a), "v"(b));
  if constexpr (I == 6) asm volatile("v_mfma_f32_16x16x32_bf16 %0, %1, %2, %0" : "+{v[216:219]}"(c) : "v"(a), "v"(b));
  if constexpr (I == 7) asm volatile("v_mfma_f32_16x16x32_bf16 %0, %1, %2, %0" : "+{v[220:223]}"(c) : "v"(a), "v"(b));
  if constexpr (I == 8) asm volatile("v_mfma_f32_16x16x32_bf16 %0, %1, %2, %0" : "+{v[224:227]}"(c) : "v"(a), "v"(b));
  if constexpr (I == 9) asm volatile("v_mfma_f32_16x16x32_bf16 %0, %1, %2, %0" : "+{v[228:231]}"(c) : "v"(a), "v"(b));
  if constexpr (I == 10) asm volatile("v_mfma_f32_16x16x32_bf16 %0, %1, %2, %0" : "+{v[232:235]}"(c) : "v"(a), "v"(b));
  if constexpr (I == 11) asm volatile("v_mfma_f32_16x16x32_bf16 %0, %1, %2, %0" : "+{v[236:239]}"(c) : "v"(a), "v"(b));
  if constexpr (I == 12) asm volatile("v_mfma_f32_16x16x32_bf16 %0, %1, %2, %0" : "+{v[240:243]}"(c) : "v"(a), "v"(b));
  if constexpr (I == 13) asm volatile("v_mfma_f32_16x16x32_bf16 %0, %1, %2, %0" : "+{v[244:247]}"(c) : "v"(a), "v"(b));
  if constexpr (I == 14) asm volatile("v_mfma_f32_16x16x32_bf16 %0, %1, %2, %0" : "+{v[248:251]}"(c) : "v"(a), "v"(b));
  if constexpr (I == 15) asm volatile("v_mfma_f32_16x16x32_bf16 %0, %1, %2, %0" : "+{v[252:255]}"(c) : "v"(a), "v"(b));
}
template <int M> __device__ __forceinline__ void pin4(u32x4 (&x)[4]) {
  if constexpr (M == 0) asm volatile("" : "+{v[64:67]}"(x[0]), "+{v[68:71]}"(x[1]), "+{v[72:75]}"(x[2]), "+{v[76:79]}"(x[3]));
  if constexpr (M == 1) asm volatile("" : "+{v[80:83]}"(x[0]), "+{v[84:87]}"(x[1]), "+{v[88:91]}"(x[2]), "+{v[92:95]}"(x[3]));
  if constexpr (M == 2) asm volatile("" : "+{v[96:99]}"(x[0]), "+{v[100:103]}"(x[1]), "+{v[104:107]}"(x[2]), "+{v[108:111]}"(x[3]));
  if constexpr (M == 3) asm volatile("" : "+{v[112:115]}"(x[0]), "+{v[116:119]}"(x[1]), "+{v[120:123]}"(x[2]), "+{v[124:127]}"(x[3]));
  if constexpr (M == 4) asm volatile("" : "+{v[128:131]}"(x[0]), "+{v[132:135]}"(x[1]), "+{v[136:139]}"(x[2]), "+{v[140:143]}"(x[3]));
  if constexpr (M == 5) asm volatile("" : "+{v[144:147]}"(x[0]), "+{v[148:151]}"(x[1]), "+{v[152:155]}"(x[2]), "+{v[156:159]}"(x[3]));
  if constexpr (M == 6) asm volatile("" : "+{v[160:163]}"(x[0]), "+{v[164:167]}"(x[1]), "+{v[168:171]}"(x[2]), "+{v[172:175]}"(x[3]));
  if constexpr (M == 7) asm volatile("" : "+{v[176:179]}"(x[0]), "+{v[180:183]}"(x[1]), "+{v[184:187]}"(x[2]), "+{v[188:191]}"(x[3]));
  if constexpr (M == 8) asm volatile("" : "+{v[192:195]}"(x[0]), "+{v[196:199]}"(x[1]), "+{v[200:203]}"(x[2]), "+{v[204:207]}"(x[3]));
  if constexpr (M == 9) asm volatile("" : "+{v[208:211]}"(x[0]), "+{v[212:215]}"(x[1]), "+{v[216:219]}"(x[2]), "+{v[220:223]}"(x[3]));
  if constexpr (M == 10) asm volatile("" : "+{v[224:227]}"(x[0]), "+{v[228:231]}"(x[1]), "+{v[232:235]}"(x[2]), "+{v[236:239]}"(x[3]));
  if constexpr (M == 11) asm volatile("" : "+{v[240:243]}"(x[0]), "+{v[244:247]}"(x[1]), "+{v[248:251]}"(x[2]), "+{v[252:255]}"(x[3]));
}
#define BAR() asm volatile("s_waitcnt lgkmcnt(0)\n\ts_barrier" ::: "memory")
#define SCHED() __builtin_amdgcn_sched_barrier(0)
#define DRAIN() asm volatile("s_waitcnt vmcnt(0)" ::: "memory")
#define RLX_AGENT __ATOMIC_RELAXED, __HIP_MEMORY_SCOPE_AGENT
constexpr int SC1 = 16;
#define BLD128(rs, vo, so) __builtin_bit_cast(u32x4, __builtin_amdgcn_raw_buffer_load_b128(rs, vo, so, 0))
#define BLD128C(rs, vo, so) __builtin_bit_cast(u32x4, __builtin_amdgcn_raw_buffer_load_b128(rs, vo, so, SC1))
#define BST128C(v, rs, vo, so) do { const u32x4 v_ = v; __builtin_amdgcn_raw_buffer_store_b128(v_, rs, vo, so, SC1); asm volatile("s_nop 1" :: "v"(v_)); } while (0)
#define ZERO4 ((f32x4){0.f, 0.f, 0.f, 0.f})

template <int I> struct IC { static constexpr int v = I; };
template <int B, int E, class F> __device__ __forceinline__ void sfor(F&& f) {
  if constexpr (B < E) { f(IC<B>{}); sfor<B + 1, E>(f); }
}
__device__ __forceinline__ rsrc_t mkrs(const void* p, unsigned bytes) {
  return __builtin_amdgcn_make_buffer_rsrc((void*)p, 0, bytes, 0x00020000);
}
__device__ __forceinline__ unsigned pack2(float a, float b) {
  f32x2 v = {a, b};
  return __builtin_bit_cast(unsigned, __builtin_convertvector(v, bf16x2));
}
__device__ __forceinline__ unsigned umin_(unsigned a, unsigned b) { return a < b ? a : b; }
__device__ __forceinline__ unsigned umax_(unsigned a, unsigned b) { return a > b ? a : b; }
__device__ __forceinline__ float bf2f(unsigned x16) { return __uint_as_float(x16 << 16); }
__device__ __forceinline__ float bfhi(unsigned x) { return __uint_as_float(x & 0xffff0000u); }
__device__ __forceinline__ float rbf(float x) { return __uint_as_float(pack2(x, 0.f) << 16); }
__device__ __forceinline__ int fresh_lane() {
  unsigned z;
  asm volatile("s_mov_b32 %0, 0" : "=s"(z));
  const int l = (int)__builtin_amdgcn_mbcnt_hi(~0u, __builtin_amdgcn_mbcnt_lo(~0u, z));
  __builtin_assume(l >= 0 && l < 64);
  return l;
}
__device__ __forceinline__ u32x4 make_sel(int c, int q) {
  u32x4 sel;
  const int e = c - 8 * (q & 1);
  const unsigned one = (e & 1) ? 0x3F800000u : 0x3F80u;
#pragma unroll
  for (int k = 0; k < 4; ++k) sel[k] = ((e >> 1) == k) ? one : 0u;
  asm volatile("s_nop 1" : "+v"(sel));
  return sel;
}
__device__ __forceinline__ int w_src(int lane, int w) {
  const int jp = lane & 3, sf = (lane >> 2) & 7, sq = (lane >> 5) + 2 * (w & 1), ss = w >> 1;
  return (sf * 128 + 32 * sq + 8 * ss + 2 * jp) * 1024;
}
__device__ __forceinline__ void stage_w(char* smem, int lane, int w, u32x4 r0, u32x4 r1) {
  const int jp = lane & 3, sf = (lane >> 2) & 7, sq = (lane >> 5) + 2 * (w & 1), ss = w >> 1;
  const int wdst = L_WB + (ss * 64 + sq * 16 + 2 * sf) * 16 + jp * 4;
#pragma unroll
  for (int h = 0; h < 8; ++h) {
    const unsigned d = __builtin_amdgcn_perm(r1[h >> 1], r0[h >> 1], (h & 1) ? 0x07060302u : 0x05040100u);
    *(unsigned*)(smem + wdst + (h & 3) * 4096 + (h >> 2) * 16) = d;
  }
}
__device__ __forceinline__ int list_off(int lane) {
  const int c = lane & 15, q = lane >> 4;
  return ((q & 1) * 2 + (c >> 3)) * 256 + ((q >> 1) * 8 + (c & 7)) * 16;
}
struct QT { u32x4 t0, t2; };
__device__ __forceinline__ void qt_init(QT& t) { asm volatile("s_nop 7" : "=v"(t.t0), "=v"(t.t2)); }
#define CNDX(perm, o, own, oth, m) do { register u64 m_ asm("vcc") = m; unsigned o_ = o; \
  asm volatile("v_cndmask_b32_dpp %0, %2, %1, vcc quad_perm:" perm " row_mask:0xf bank_mask:0xf" : "+v"(o_) : "v"(own), "v"(oth), "s"(m_)); o = o_; } while (0)
#define CNDXI(perm, own, oth, m) do { register u64 m_ asm("vcc") = m; unsigned o_ = own; \
  asm volatile("v_cndmask_b32_dpp %0, %1, %0, vcc quad_perm:" perm " row_mask:0xf bank_mask:0xf" : "+v"(o_) : "v"(oth), "s"(m_)); own = o_; } while (0)
__device__ __forceinline__ void qt_step(u32x4 (&x)[4], QT& t, int n) {
  const int st = n >> 3, p = (n >> 2) & 1, d = n & 3;
  if (st == 0) {
    if (p == 0) CNDX("[1,0,3,2]", t.t0[d], x[0][d], x[1][d], 0x5555555555555555ull);
    else        CNDX("[1,0,3,2]", t.t2[d], x[2][d], x[3][d], 0x5555555555555555ull);
  } else if (st == 1) {
    if (p == 0) CNDXI("[1,0,3,2]", x[3][d], x[2][d], 0xAAAAAAAAAAAAAAAAull);
    else        CNDXI("[1,0,3,2]", x[1][d], x[0][d], 0xAAAAAAAAAAAAAAAAull);
  } else if (st == 2) {
    if (p == 0) CNDX("[2,3,0,1]", x[0][d], t.t0[d], t.t2[d], 0x3333333333333333ull);
    else        CNDX("[2,3,0,1]", x[2][d], x[1][d], x[3][d], 0x3333333333333333ull);
  } else {
    if (p == 0) CNDXI("[2,3,0,1]", t.t2[d], t.t0[d], 0xCCCCCCCCCCCCCCCCull);
    else        CNDXI("[2,3,0,1]", x[3][d], x[1][d], 0xCCCCCCCCCCCCCCCCull);
  }
  if (n == 31) { const u32x4 dead = x[1]; x[1] = x[2]; x[2] = t.t2; t.t2 = dead; asm volatile("s_nop 1"); SCHED(); }
}
__device__ __forceinline__ void qt_all(u32x4 (&x)[4], QT& t) {
#pragma unroll
  for (int n = 0; n < 32; ++n) qt_step(x, t, n);
}
__device__ __forceinline__ void wait_flags(const char* p, u64 tag) {
  gu64* f = (gu64*)(p + (fresh_lane() & 31) * 8);
#pragma unroll 1
  for (int spins = 0; spins < (1 << 16); ++spins) {
    const u64 v = __hip_atomic_load(f, RLX_AGENT);
    if (__all(v == tag)) break;
    __builtin_amdgcn_s_sleep(2);
  }
  __builtin_amdgcn_fence(__ATOMIC_ACQUIRE, "wavefront");
}
__device__ __forceinline__ float gelu_tanh(float x) {
  const float u = 0.7978845608028654f * (x + 0.044715f * x * x * x);
  const float e = __expf(-2.f * u);
  return x * __builtin_amdgcn_rcpf(1.f + e);
}

__device__ __forceinline__ float row_sum(float x) {
  x += __uint_as_float(__builtin_amdgcn_mov_dpp(__float_as_uint(x), 0xB1, 0xf, 0xf, true));
  x += __uint_as_float(__builtin_amdgcn_mov_dpp(__float_as_uint(x), 0x4E, 0xf, 0xf, true));
  x += __uint_as_float(__builtin_amdgcn_mov_dpp(__float_as_uint(x), 0x141, 0xf, 0xf, true));
  x += __uint_as_float(__builtin_amdgcn_mov_dpp(__float_as_uint(x), 0x140, 0xf, 0xf, true));
  return x;
}

#define E2CAP 18
#define PRIO 1
#define WTD 4
#define E2G 8
#define PREPW 0
#define NB1 6
#define WLK 5
#define P1 5
#ifndef NVI
#define NVI 30
#endif
#define UXLD 6
#define IXD 5
#define TPRE 3
#define TPRE0 1
#define GDEP 4

constexpr int e2cnt(int u) { return u <= 21 ? (E2G + u < E2CAP ? E2G + u : E2CAP) : E2CAP + 2 * (u - 21); }

__global__ void __launch_bounds__(512, 2)
k_fused(const int* __restrict__ idx, const char* __restrict__ table, const u16* __restrict__ wp,
        const char* __restrict__ w1, const u16* __restrict__ b1, const u16* __restrict__ w2,
        const u16* __restrict__ b2, char* __restrict__ ws, u16* __restrict__ out) {
  __shared__ __attribute__((aligned(16))) char smem[L_TOTAL];
  const int w = __builtin_amdgcn_readfirstlane(threadIdx.x >> 6);
  const int bid = blockIdx.x, xl = bid & 7, rr = bid >> 3;
  const int g = xl * 8 + (rr & 7), phi = rr >> 3;
  const rsrc_t rs_w1 = mkrs(w1, NF * ND * NH * 2), rs_tab = mkrs(table, NV * ND * 2), rs_ws = mkrs(ws, WS_END);
  const rsrc_t rs_idx = mkrs(idx, NB * NF * 4);
  const int wsb = phi * 24 * 128 * 1024 + g * 16;
  const int lsb = WS_LIST + (phi * 3 * 256 + w * 32) * 256;
  const unsigned long long t00 = __builtin_amdgcn_s_memrealtime(), c00 = __builtin_amdgcn_s_memtime(); unsigned long long tk = t00;

  const u64 tag = *(const __attribute__((address_space(4))) u64*)(ws + WS_EPOCH) * 6364136223846793005ull + 1442695040888963407ull;
  u32x4 T[8][4];
  const int tsb = w * 128 * 256, slice = L_P + w * 16384;
  unsigned dv[4];
  auto dma_t = [&](auto M, auto I) {
    constexpr int m = M.v, i = I.v, slot = m < 4 ? m : m - 3;
    dma16<i * 1024>(dv[i], slice + slot * 4096, rs_tab, tsb + m * 4096);
  };
  u32x4 pi[6];
  unsigned la, lb, wa, wb;
  {
    const int lane = fresh_lane();
    if (w == PREPW) {
      const int c7 = lane & 7, po = c7 * (NF * 4) + (lane >> 4) * 32 + (((lane >> 3) ^ (lane >> 2)) & 1) * 16;
      sfor<0, 6>([&](auto K) { ld16<(K.v & 1) * (8 * NF * 4) + (K.v >> 1) * 128>(pi[K.v], po, rs_idx, bid * 16 * NF * 4); });
      asm volatile("buffer_load_dword %0, %2, %3, %4 offen offset:384\n\tbuffer_load_dword %1, %2, %3, %4 offen offset:3584"
                   : "=&v"(la), "=&v"(lb) : "v"(c7 * (NF * 4) + ((lane >> 3) & 3) * 4), "s"(rs_idx), "s"(bid * 16 * NF * 4));
    }
    u32x4 wp0, wp1;
    asm volatile("buffer_load_dwordx4 %0, %2, %3, %4 offen\n\tbuffer_load_dwordx4 %1, %2, %3, %4 offen offset:1024"
                 : "=&v"(wp0), "=&v"(wp1) : "v"(w_src(lane, w)), "s"(rs_w1), "s"(wsb));
    SCHED();
    if (w == 0 && lane == 0) *(unsigned*)(smem + L_MISC) = 0u;
    {
      const int h = (lane & 15) ^ (lane >> 4);
      dv[0] = (lane >> 4) * 256 + (h ^ ((h & 4) << 1)) * 16;
      dv[1] = dv[0] ^ 192; dv[2] = dv[0] ^ 128; dv[3] = dv[0] ^ 64;
    }
    SCHED();
    asm volatile("s_barrier" ::: "memory");
    if (w == PREPW) {
      const rsrc_t rs_wp = mkrs(wp, NF * NF * 2);
      const int id = bid * 23 + lane, tile = id >> 8, l = (id >> 2) & 63, r = id & 3, c = l & 15, q = l >> 4;
      int tj = (tile >= 1) + (tile >= 3) + (tile >= 6) + (tile >= 10) + (tile >= 15);
      int i = 16 * (tile - tj * (tj + 1) / 2) + 4 * q + r, j = 16 * tj + c;
      bool oka = true, okb = (i >> 4) != tj;
      if (tile >= 21) {
        tj = (tile - 21) * 4 + r; i = 96 + q; j = tj < 6 ? 16 * tj + c : 96 + (c >> 2);
        oka = tj < 6 || (tj == 6 && (c & 3) == 0); okb = tj < 6;
      }
      asm volatile("buffer_load_ushort %0, %2, %4, 0 offen\n\tbuffer_load_ushort %1, %3, %4, 0 offen"
                   : "=&v"(wa), "=&v"(wb) : "v"((oka && lane < 23) ? (i * NF + j) * 2 : 0x100000), "v"((okb && lane < 23) ? (j * NF + i) * 2 : 0x100000), "s"(rs_wp));
      asm volatile("s_waitcnt vmcnt(4)" : "+v"(la), "+v"(lb), "+v"(pi[0]), "+v"(pi[1]), "+v"(pi[2]), "+v"(pi[3]), "+v"(pi[4]), "+v"(pi[5]));
      const int jb = (((lane >> 3) ^ (lane >> 2)) & 1) * 4;
      const unsigned k0 = jb * 0x10001u + 0x10000u, k1 = k0 + 0x20002u, sh = (lane & 1) * 16;
      const bool sw = lane & 2;
#pragma unroll
      for (int k = 0; k < 3; ++k) {
        u32x4 o;
#pragma unroll
        for (int tp = 0; tp < 2; ++tp) {
          const u32x4 a = pi[2 * k + tp];
          const unsigned p0 = ((a[0] | (a[1] << 16)) << 3) + k0, p1 = ((a[2] | (a[3] << 16)) << 3) + k1;
          const unsigned r0 = __builtin_amdgcn_alignbit(p0, p0, sh), r1 = __builtin_amdgcn_alignbit(p1, p1, sh);
          o[2 * tp] = sw ? r1 : r0; o[2 * tp + 1] = sw ? r0 : r1;
        }
        BST128C(o, rs_ws, (lane >> 4) * 65536 + (lane & 15) * 16, WS_LIST + k * 4 * 65536 + bid * 256);
      }
      __builtin_amdgcn_raw_buffer_store_b32(la | (lb << 16), rs_ws, lane < 32 ? (lane >> 3) * 8192 + (lane & 7) * 16 : 0x7f000000,
                                            WS_LX + (bid >> 5) * 1024 + (bid & 7) * 128 + ((bid >> 3) & 3) * 4, SC1);
    }
    sfor<0, 4>([&](auto I) { dma_t(IC<0>{}, I); });
    if (w == PREPW) asm volatile("s_waitcnt vmcnt(10)" ::: "memory"); else asm volatile("s_waitcnt vmcnt(4)" ::: "memory");
    asm volatile("" : "+v"(wp0), "+v"(wp1) :: "memory");
    stage_w(smem, lane, w, wp0, wp1);
    if (w == PREPW) {
      asm volatile("s_waitcnt vmcnt(8)" : "+v"(wa), "+v"(wb));
      __builtin_amdgcn_raw_buffer_store_b32(__float_as_uint(bf2f(wa) + bf2f(wb)), rs_ws, lane < 23 ? lane * 4 : 0x7f000000, WS_WT + bid * 92, SC1);
    }
    SCHED();
    sfor<0, 4>([&](auto I) { dma_t(IC<1>{}, I); });
    sfor<0, 4>([&](auto I) { dma_t(IC<2>{}, I); });
  }
  STAMP(1);
  BAR();
  if (w == PREPW) {
    DRAIN();
    if (fresh_lane() == 0) __hip_atomic_store((gu64*)(ws + WS_F1 + bid * 8), tag, RLX_AGENT);
  }
  STAMP(2);
  if (P1 && w >= 4) __builtin_amdgcn_s_setprio(1);
  auto touch_l = [&](int ph) -> unsigned {
    const int lane = fresh_lane();
    return __builtin_amdgcn_raw_buffer_load_b32(rs_ws, lane < 8 ? ((rr & 7) * 8 + lane) * 128 : 0x7f000000, lsb + ph * 65536, SC1);
  };

  u32x4 ix[IXD];
  auto load_ix = [&](int ph, int k0, int k1) {
    const int lsrc = list_off(fresh_lane());
#pragma unroll
    for (int k = k0; k < k1; ++k) ix[k] = BLD128C(rs_ws, lsrc + (k & 3) * 1024, lsb + ph * 65536 + (k >> 2) * 4096);
  };
  f32x4 acc[16];
  {
    const int lane = fresh_lane(), c = lane & 15, q = lane >> 4;
    int pw = L_P + (128 * w + 4 * q) * 128 + c * 8;
    u32x4 Wf[4][4];
#pragma unroll
    for (int t = 0; t < 4; ++t)
#pragma unroll
      for (int s = 0; s < 4; ++s) Wf[t][s] = *(const u32x4*)(smem + L_WB + (t * 4 + s) * 1024 + lane * 16);
    SCHED();
    const int toff = ((lane >> 2) & 3) * 1024 + (lane >> 4) * 64 + (lane & 3) * 16;
    int ra[4];
    ra[0] = slice + c * 256 + (((q ^ ((q & 1) << 1)) << 2) ^ c) * 16;
    ra[1] = ra[0] ^ 16; ra[2] = ra[0] ^ 32; ra[3] = ra[0] ^ 48;
    asm volatile("" : "+v"(ra[0]), "+v"(ra[1]), "+v"(ra[2]), "+v"(ra[3]), "+v"(pw));
    auto read_t = [&](int m, int s) { T[m][s] = *(const u32x4*)(smem + ra[s] + (m < 4 ? m : m - 3) * 4096); };
    auto load_t = [&](auto M, auto I) {
      ld16<I.v * 256>(T[M.v][I.v], toff, rs_tab, tsb + M.v * 4096);
    };
    asm volatile("s_waitcnt vmcnt(8)" ::: "memory");
#pragma unroll
    for (int s = 0; s < 4; ++s) read_t(0, s);
    u64 fl;
    u32x2 bz;
    u32x4 xr0, xr1;
    QT qt; qt_init(qt);
    f32x4 D[2][2];
    RESERVE(D[0][0]); RESERVE(D[0][1]); RESERVE(D[1][0]); RESERVE(D[1][1]);
    u32x2 o[4];
    SCHED();
    auto block = [&](auto M) {
      constexpr int m = M.v;
      if constexpr (m == P1) __builtin_amdgcn_s_setprio(0);
      sfor<0, 16>([&](auto K) {
        constexpr int k = K.v, h = k >> 3, s = (k >> 1) & 3, t = k & 1;
        if constexpr (m == 8 && k == 0) asm volatile("s_nop 7");
        if constexpr ((m == 3 || m == 6) && k == 0)
          wait4<(m == 3 ? 4 : 0)>(T[m + 1]);
        if constexpr (m < 8) mf<s == 0>(D[h][t], T[m][s], Wf[2 * h + t][s]);
        SCHED();
        if constexpr (m == 3 || m == 6) { qt_step(T[m + 1], qt, 2 * k); qt_step(T[m + 1], qt, 2 * k + 1); }
        if constexpr ((k & 3) == 1 && m < 5) {
          if constexpr (m == 1 || m == 4) load_t(IC<m + 3>{}, IC<(k >> 2)>{}); else dma_t(IC<m + 3>{}, IC<(k >> 2)>{});
        }
        if constexpr (m != 3 && m < 6 && k >= 8 && k < 12) {
          if constexpr (k == 8) asm volatile("s_waitcnt vmcnt(%0)" :: "i"(m < 5 ? 6 : 4) : "memory");
          read_t(m + 1, k - 8);
        }
        if constexpr ((m == 1 || m == 2) && k == 15) asm volatile("s_waitcnt lgkmcnt(0)" ::: "memory");
        if constexpr (m == 6 && k == 1) {
          fl = __hip_atomic_load((gu64*)(ws + WS_F1 + w * 256 + (fresh_lane() & 31) * 8), RLX_AGENT);
          bz = __builtin_amdgcn_raw_buffer_load_b64(mkrs(b1, phi == 0 ? NH * 2 : 0), ((fresh_lane() >> 4) & 1) * 8, g * 16, 0);
          if (w == 7) {
            const int l2 = fresh_lane();
            xr0 = BLD128(rs_w1, l2 * 2048, (96 + phi) * 128 * 1024 + g * 16);
            xr1 = BLD128(rs_w1, l2 * 2048 + 1024, (96 + phi) * 128 * 1024 + g * 16);
          }
        }
        if constexpr (m == 7 && k == 0) {
          if (!__all(fl == tag)) wait_flags(ws + WS_F1 + w * 256, tag);
          __builtin_amdgcn_fence(__ATOMIC_ACQUIRE, "wavefront");
          load_ix(0, 0, IXD);
        }
        if constexpr (m > 0 && k >= 4 && k < 8) {
          constexpr int r = k - 4;
          o[r][1] = pack2(D[1][0][r], D[1][1][r]);
          *(u32x2*)(smem + pw + (m - 1) * 2048 + r * 128) = o[r];
        }
        if constexpr (m < 8 && k >= 12) o[k - 12][0] = pack2(D[0][0][k - 12], D[0][1][k - 12]);
        SCHED();
      });
    };
    sfor<0, 8>(block);
    if (w < 7) block(IC<8>{});
    else {
      const int l2 = fresh_lane(), xd = L_WX + (((l2 >> 2) & 3) * 4 + (l2 >> 4)) * 128 + (l2 & 3) * 4;
#pragma unroll
      for (int h = 0; h < 8; ++h) {
        const unsigned d = __builtin_amdgcn_perm(xr1[h >> 1], xr0[h >> 1], (h & 1) ? 0x07060302u : 0x05040100u);
        *(unsigned*)(smem + xd + h * 16) = d;
      }
    }
    const f32x4 a0 = {bf2f(bz[0]), bfhi(bz[0]), bf2f(bz[1]), bfhi(bz[1])};
#pragma unroll
    for (int i = 0; i < 16; ++i) acc[i] = a0;
  }
  STAMP(3);
  BAR();
  STAMP(4);

  auto blank = [&](int k0, int k1) {
    u32x4 ones = {~0u, ~0u, ~0u, ~0u};
    opaque(ones);
    const int l16 = fresh_lane() * 16;
#pragma unroll
    for (int k = k0; k < k1; ++k) BST128C(ones, rs_ws, l16 + (k & 3) * 1024, WS_X + bid * 65536 + w * 16384 + (k >> 2) * 4096);
  };
  sfor<0, 2>([&](auto PH) {
    constexpr int ph = PH.v;
    sfor<0, 8>([&](auto M) { pin4<M.v>(T[M.v]); });
    u32x4 lx;
    {
      const int lane = fresh_lane(), c = lane & 15, q = lane >> 4;
      const int lsrc = list_off(lane);
      u32x4 wr0, wr1;
      auto load_w = [&](int v) {
        const int wsrc = w_src(fresh_lane(), v);
        wr0 = BLD128(rs_w1, wsrc, wsb + (ph + 1) * 8 * 128 * 1024); wr1 = BLD128(rs_w1, wsrc + 1024, wsb + (ph + 1) * 8 * 128 * 1024);
      };
      const u32x4 sel = make_sel(c, q);
      constexpr int DEP = GDEP;
      u32x4 gv[DEP];
      auto gaddr = [&](int n) -> unsigned {
        const unsigned x = ix[(n >> 3) % IXD][(n >> 1) & 3];
        return ((n & 1) ? (x >> 16) : (x & 0xffffu)) << 4;
      };
      u32x4 Wx[4];
      f32x4 dx[2];
      int xw = L_PX + (128 * w + c) * 16 + (q & 1) * 8;
      if constexpr (ph == 0) {
#pragma unroll
        for (int s = 0; s < 4; ++s) Wx[s] = *(const u32x4*)(smem + L_WX + (s * 4 + q) * 128 + (c & 7) * 16);
        RESERVE(dx[0]); RESERVE(dx[1]);
        asm volatile("" : "+v"(xw));
      }
#pragma unroll
      for (int n = 0; n < DEP; ++n) gv[n] = *(const u32x4*)(smem + L_P + gaddr(n));
      sfor<0, 64>([&](auto N) {
        constexpr int n = N.v;
        if constexpr (ph == 0 && n < 32) mf<(n & 3) == 0>(dx[(n >> 2) & 1], Wx[n & 3], T[n >> 2][n & 3]);
        if constexpr (ph == 0 && n >= 4 && n < 36 && (n & 3) == 3) {
          constexpr int m = (n >> 2) - 1;
          SCHED();
          if (m < 7 || w < 7) *(u32x2*)(smem + xw + m * 256) = (u32x2){pack2(dx[m & 1][0], dx[m & 1][1]), pack2(dx[m & 1][2], dx[m & 1][3])};
          SCHED();
        }
        if constexpr (ph == 0 && (n & 3) == 0) acc[n >> 2] = MFMA(sel, gv[n % DEP], acc[n >> 2]); else mfma_acc<(n >> 2)>(acc[n >> 2], sel, gv[n % DEP]);
        SCHED();
        if (n + DEP < 64) gv[n % DEP] = *(const u32x4*)(smem + L_P + gaddr(n + DEP));
        if (((n + DEP) & 7) == 7) {
          const int k = ((n + DEP) >> 3) + IXD;
          if (k < 8) ix[k % IXD] = BLD128C(rs_ws, lsrc + (k & 3) * 1024, lsb + ph * 65536 + (k >> 2) * 4096);
          if (k == WLK && w < 4) load_w(w);
          if (k == WLK + 3 && w < 4) { stage_w(smem, fresh_lane(), w, wr0, wr1); load_w(w + 4); }
          if (k == 10 && ph == 0 && w >= 4) lx = BLD128C(rs_ws, fresh_lane() * 16, WS_LX + phi * 8192 + w * 1024);
        }
        SCHED();
      });
      if (w < 4) {
        const int l2 = fresh_lane();
        u32x4 wt[4];
        if constexpr (ph == 1) {
#pragma unroll
          for (int k = 0; k < 4; ++k) wt[k] = BLD128C(rs_ws, l2 * 16 + k * 1024, WS_WT + w * 4096);
        }
        stage_w(smem, l2, w + 4, wr0, wr1);
        if constexpr (ph == 0) blank(0, NB1);
        else {
#pragma unroll
          for (int k = 0; k < 4; ++k) if (k < 3 || w < 3) *(u32x4*)(smem + L_WT + w * 4096 + k * 1024 + l2 * 16) = wt[k];
          DRAIN();
        }
      }
    }
    STAMPL(5);
    BAR();
    if (ph == 1 && w == 0 && fresh_lane() == 0) __hip_atomic_store((gu64*)(ws + WS_F2 + bid * 8), tag, RLX_AGENT);
    STAMPL(6);
    auto extra_gather = [&]() {
      const int lane = fresh_lane(), c = lane & 15, q = lane >> 4;
      u32x4 gv[8];
#pragma unroll
      for (int n = 0; n < 8; ++n) gv[n] = *(const u32x4*)(smem + L_PX + (((n & 1) ? (lx[n >> 1] >> 16) : (lx[n >> 1] & 0xffffu)) << 4));
      SCHED();
      const u32x4 sel = make_sel(c, q);
      u32x4 slo = q < 2 ? sel : (u32x4){0, 0, 0, 0}, shi = q < 2 ? (u32x4){0, 0, 0, 0} : sel;
      asm volatile("s_nop 1" : "+v"(slo), "+v"(shi));
      SCHED();
      sfor<0, 8>([&](auto N) {
        constexpr int n = N.v, a = 2 * (n & 6) + (n & 1);
        mfma_acc<a>(acc[a], slo, gv[n]);
        mfma_acc<a + 2>(acc[a + 2], shi, gv[n]);
      });
    };
    if constexpr (ph == 0) { if (w >= 4) extra_gather(); }
    SCHED();
    sfor<0, 8>([&](auto M) { pin4<M.v>(T[M.v]); });
    {
      const int lane = fresh_lane(), c = lane & 15, q = lane >> 4;
      int pw = L_P + (128 * w + 4 * q) * 128 + c * 8, wb = L_WB + lane * 16, eo;
      asm volatile("" : "+v"(pw), "+v"(wb));
      u32x4 Wf[2][4];
#pragma unroll
      for (int t = 0; t < 2; ++t)
#pragma unroll
        for (int s = 0; s < 4; ++s) Wf[t][s] = *(const u32x4*)(smem + wb + (t * 4 + s) * 1024);
      f32x4 D[2][2];
      RESERVE(D[0][0]); RESERVE(D[0][1]); RESERVE(D[1][0]); RESERVE(D[1][1]);
      unsigned tl, pk[4];
      auto um = [](int u) { return ph ? (u >> 2) * 2 + (u & 1) : (u & 7); };
      auto uh = [](int u) { return ph ? ((u >> 1) ^ (u >> 2)) & 1 : (u >> 3); };
      sfor<0, 18>([&](auto U) {
        constexpr int u = U.v, m = um(u), h = uh(u);
        sfor<0, 8>([&](auto J) {
          constexpr int j = J.v, s = j >> 1, t = j & 1;
          if constexpr (u < 16) {
            mf<s == 0>(D[u & 1][t], T[m][s], Wf[t][s]);
            SCHED();
            if constexpr (u < 15 && uh(u + 1) != h) Wf[t][s] = *(const u32x4*)(smem + wb + (((h ^ 1) * 2 + t) * 4 + s) * 1024);
          }
          if constexpr (u >= 2 && j < 2) {
            *(unsigned*)(smem + pw + j * 256) = pk[2 * j];
            *(unsigned*)(smem + pw + j * 256 + 128) = pk[2 * j + 1];
          }
          if constexpr (u >= 2 && u < 17 && j == 2) { pw += (um(u - 1) - um(u - 2)) * 2048 + (uh(u - 1) - uh(u - 2)) * 4; asm volatile("" : "+v"(pw)); }
          if constexpr (u == 1 && j == 3) asm volatile("s_nop 2");
          if constexpr (u == 16 && j == 3) asm volatile("s_nop 3");
          if constexpr (u >= 1 && u < 17 && j >= 4) pk[j - 4] = pack2(D[(u - 1) & 1][0][j - 4], D[(u - 1) & 1][1][j - 4]);
          if constexpr (u == 2 && j == 3) tl = touch_l(ph + 1);
          if constexpr (ph == 1) {
            if constexpr (u == 4 && j < 7) {
              if constexpr (j == 0) { eo = ((wb - L_WB) >> 2) & 48; asm volatile("" : "+v"(eo)); }
              if constexpr (j < 6) T[j >> 2][j & 3] = BLD128(rs_idx, eo + j * 64, (bid * 16 + w * 2) * NF * 4);
              else {
                T[1][2][0] = __builtin_amdgcn_raw_buffer_load_b32(rs_idx, (eo >> 2) + 384, (bid * 16 + w * 2) * NF * 4, 0);
                eo = (((wb - L_WB) >> 2) & 192) + ((wb - L_WB) & 48); asm volatile("" : "+v"(eo));
              }
            }
            if constexpr ((u == 7 || u == 8 || u == 11 || u == 12 || u == 15 || u == 16) && (j & 1) == 1) {
              constexpr int b = (u - 6) >> 1;
              T[b + 2][s] = BLD128(rs_tab, T[b >> 2][b & 3][s] * 256 + eo, 0);
            }
            if constexpr (u == 17 && j == 0) T[1][3] = BLD128(rs_tab, T[1][2][0] * 256 + eo, 0);
            if constexpr (u == 10 && j == 3) T[0][0] = BLD128C(rs_ws, wb - L_WB, WS_WT + (15 + w) * 1024);
            if constexpr (u == 14 && j == 3) {
              const u32x2 f = __builtin_amdgcn_raw_buffer_load_b64(rs_ws, ((wb - L_WB) >> 1) & 255, WS_F2 + w * 256, SC1);
              T[0][3][0] = f[0]; T[0][3][1] = f[1];
            }
          }
          if constexpr (u == 12 && j == 3) { asm volatile("" :: "v"(tl)); load_ix(ph + 1, 0, 1); }
          if constexpr (u == 15 && j == 7) {
            if constexpr (ph == 0) { if (w < 4) lx = BLD128C(rs_ws, wb - L_WB, WS_LX + phi * 8192 + w * 1024); }
            load_ix(ph + 1, 1, IXD);
          }
          SCHED();
        });
      });
    }
    if constexpr (ph == 0) { if (w < 4) { blank(NB1, 16); extra_gather(); } }
    STAMPL(7);
    BAR();
    STAMPL(8);
  });

  if (PRIO && w >= 4) __builtin_amdgcn_s_setprio(1);
  u32x4 E[7][4], E2[7][4], vi[7];
  int eoff;
  auto load_e2 = [&](auto J) {
    constexpr int k = J.v;
    if constexpr (k < 24 && (k & 3) == 0) pin1<16 + (k >> 2)>(vi[k >> 2]);
    if constexpr (k < 24) E2[k >> 2][k & 3] = BLD128(rs_tab, vi[k >> 2][k & 3] * 256 + eoff, 0);
  };
  QT qt;
  {
    const int lane = fresh_lane(), c = lane & 15, q = lane >> 4;
    const int lsrc = list_off(lane);
    u32x4 jx[8];
#pragma unroll
    for (int t = 0; t < 6; ++t)
#pragma unroll
      for (int i = 0; i < 4; ++i) E[t][i] = T[t + 2][i];
    E[6][0] = T[1][3];
#pragma unroll
    for (int k = 0; k < IXD; ++k) jx[k] = ix[k];
#pragma unroll
    for (int k = IXD; k < 8; ++k) jx[k] = BLD128C(rs_ws, lsrc + (k & 3) * 1024, lsb + 2 * 65536 + (k >> 2) * 4096);
    const u32x4 sel = make_sel(c, q);
    const int xo = ((q >> 1) * 2 + (c >> 3)) * 65536 + ((q & 1) * 8 + (c & 7)) * 16;
    const int xb = WS_X + (w * 32 * 256 + g * 4 + phi) * 256;
    constexpr int DEP = GDEP;
    u32x4 gv[DEP];
    auto gaddr = [&](int n) -> unsigned {
      const unsigned x = jx[n >> 3][(n >> 1) & 3];
      return ((n & 1) ? (x >> 16) : (x & 0xffffu)) << 4;
    };
#pragma unroll
    for (int n = 0; n < DEP; ++n) gv[n] = *(const u32x4*)(smem + L_P + gaddr(n));
    *(u32x4*)(smem + L_WT + (15 + w) * 1024 + lane * 16) = T[0][0];
    asm volatile("" ::: "memory");
    if (lane == 0) __hip_atomic_fetch_add((unsigned*)(smem + L_MISC), 1u, __ATOMIC_RELAXED, __HIP_MEMORY_SCOPE_WORKGROUP);
    if (!__all((((u64)T[0][3][1] << 32) | T[0][3][0]) == tag)) wait_flags(ws + WS_F2 + w * 256, tag);
    qt_init(qt);
    SCHED();
    sfor<0, 68>([&](auto N) {
      constexpr int n = N.v;
      if constexpr (n < 64) mfma_acc<(n >> 2)>(acc[n >> 2], sel, gv[n % DEP]);
      SCHED();
      if constexpr (n + DEP < 64) gv[n % DEP] = *(const u32x4*)(smem + L_P + gaddr(n + DEP));
      if constexpr (n == 64) asm volatile("s_nop 3" : "+{v[92:95]}"(E[6][0]));
      if constexpr (n >= 64 - 2 * E2G && n < 64 && (n & 1) == 0) load_e2(IC<(n - 64 + 2 * E2G) / 2>{});
      if constexpr (n == NVI) {
        eoff = (lane >> 4) * 64 + (lane & 3) * 16;
#pragma unroll
        for (int t = 0; t < 6; ++t) vi[t] = BLD128(rs_idx, ((lane >> 2) & 3) * 16 + t * 64, (bid * 16 + w * 2 + 1) * NF * 4);
      }
      if constexpr (n >= 11 && (n & 7) == 3) {
        constexpr int ch = (n >> 3) - 1;
        const f32x4 a = acc[2 * ch], b = acc[2 * ch + 1];
        BST128C(((u32x4){umin_(pack2(a[0], a[1]), ~1u), umin_(pack2(a[2], a[3]), ~1u), umin_(pack2(b[0], b[1]), ~1u), umin_(pack2(b[2], b[3]), ~1u)}), rs_ws, xo, xb + ch * 4 * 65536);
      }
      if constexpr (n < 64) sfor<3 * n, 3 * n + 3>([&](auto K) { if constexpr ((K.v & 31) == 0) pin4<(K.v >> 5) + 2>(E[K.v >> 5]); qt_step(E[K.v >> 5], qt, K.v & 31); });
      SCHED();
    });
  }
  if (PRIO == 2) __builtin_amdgcn_s_setprio(0);
  STAMP(13);

#pragma unroll 1
  for (int spins = 0; spins < (1 << 20); ++spins) {
    if (__builtin_amdgcn_readfirstlane(__hip_atomic_load((unsigned*)(smem + L_MISC), __ATOMIC_RELAXED, __HIP_MEMORY_SCOPE_WORKGROUP)) == 8u) break;
    __builtin_amdgcn_s_sleep(1);
  }
  asm volatile("" ::: "memory");
  STAMP(14);
  u32x4 X[8];
  u32x2 ww[4];
  auto pnn = [&](u32x4 (&Ee)[7][4], u32x4 (&En)[7][4], auto nxt, auto hook) -> float {
    int wa = L_WT + fresh_lane() * 16;
    asm volatile("" : "+v"(wa));
    f32x4 sm = ZERO4, D[3], wt[WTD];
    RESERVE(D[0]); RESERVE(D[1]); RESERVE(D[2]);
    sfor<0, WTD - 2>([&](auto J) { wt[J.v] = *(const f32x4*)(smem + wa + J.v * 1024); });
    sfor<0, 30>([&](auto U) {
      constexpr int u = U.v;
      constexpr int tj = u < 1 ? 0 : u < 3 ? 1 : u < 6 ? 2 : u < 10 ? 3 : u < 15 ? 4 : u < 21 ? 5 : 6, ti = u - tj * (tj + 1) / 2;
      if constexpr (u >= 1 && u + WTD - 3 < 23) wt[(u + WTD - 3) % WTD] = *(const f32x4*)(smem + wa + (u + WTD - 3) * 1024);
      sfor<0, 4>([&](auto S) {
        constexpr int s = S.v, k = u * 4 + s;
        if constexpr (u < 21) mf<s == 0>(D[u % 3], Ee[ti][s], Ee[tj][s]);
        else if constexpr (u < 28) mf<s == 0>(D[u % 3], Ee[6][s], Ee[ti][s]);
        else if constexpr (s == 0) asm volatile("s_nop 3");
        SCHED();
        constexpr int b = k < 12 ? 2 : k < 24 ? 3 : k < 40 ? 4 : k < 60 ? 5 : 6, k0 = k < 12 ? 0 : k < 24 ? 12 : k < 40 ? 24 : 40, nm = k < 24 ? 12 : k < 40 ? 16 : 20;
        if constexpr (!nxt.v && b < 6) sfor<((k - k0) * 32) / nm, ((k - k0 + 1) * 32) / nm>([&](auto N) { if constexpr (N.v == 0) pin4<8 + b>(Ee[b]); qt_step(Ee[b], qt, N.v); });
        if constexpr (k >= 64 && k < 80) {
          constexpr int j = 3 - ((k - 64) >> 2), d = k & 3;
          Ee[6][j][d] = __builtin_amdgcn_mov_dpp(Ee[6][0][d], j * 0x55, 0xf, 0xf, true);
        }
        if constexpr (nxt.v && k >= 80 && k < 112) { if constexpr ((k & 15) == 0) pin4<8 + ((k - 80) >> 4)>(En[(k - 80) >> 4]); qt_step(En[(k - 80) >> 4], qt, (2 * k) & 31); qt_step(En[(k - 80) >> 4], qt, (2 * k + 1) & 31); }
        if constexpr (u >= 2 && u - 2 < 21) { sm[s] += D[(u - 2) % 3][s] * wt[(u - 2) % WTD][s]; asm volatile("" : "+v"(sm[s])); }
        if constexpr (u - 2 >= 21 && s == 3) { sm[0] += D[(u - 2) % 3][0] * wt[(21 + ((u - 2 - 21) >> 2)) % WTD][(u - 2 - 21) & 3]; asm volatile("" : "+v"(sm[0])); }
        SCHED();
      });
      hook(U);
    });
    return row_sum((sm[0] + sm[1]) + (sm[2] + sm[3]));
  };
  auto load_x = [&]() {
    const int lane = fresh_lane();
#pragma unroll
    for (int k = 0; k < 8; ++k) X[k] = BLD128C(rs_ws, lane * 16 + (k & 3) * 1024, WS_X + (bid * 256 + w * 32) * 256 + (k >> 2) * 4096);
  };
  const float lp0 = pnn(E, E2, IC<1>{}, [&](auto U) {
    if constexpr (U.v == 21) vi[6][0] = __builtin_amdgcn_raw_buffer_load_b32(rs_idx, (fresh_lane() & 12) + 384, (bid * 16 + w * 2 + 1) * NF * 4, 0);
    sfor<e2cnt(U.v), e2cnt(U.v + 1)>(load_e2);
    if constexpr (U.v == 29) E2[6][0] = BLD128(rs_tab, vi[6][0] * 256 + eoff, 0);
    SCHED();
  });
  if (PRIO == 1) __builtin_amdgcn_s_setprio(0);
  STAMP(15);
  const float lp1 = pnn(E2, E2, IC<0>{}, [&](auto U) {
    if constexpr (U.v == UXLD) {
      const int lane = fresh_lane();
#pragma unroll
      for (int j = 0; j < 4; ++j) ww[j] = *(const u32x2*)(w2 + (8 * w + 2 * j + (lane >> 5)) * 8 + ((lane >> 3) & 1) * 4);
      load_x();
      SCHED();
    }
  });
#pragma unroll 1
  for (int spins = 0; spins < (1 << 14); ++spins) {
    unsigned mx = 0;
#pragma unroll
    for (int k = 0; k < 8; ++k) mx = umax_(umax_(umax_(mx, X[k][0]), X[k][1]), umax_(X[k][2], X[k][3]));
    if (!__any(mx == ~0u)) break;
    __builtin_amdgcn_s_sleep(4);
    asm volatile("" ::: "memory");
    load_x();
  }
  STAMP(16);

  {
    const int lane = fresh_lane(), c = lane & 15;
    const u32x4 slo = make_sel(c, 0), shi = make_sel(c, 1);
    float s0 = 0.f;
#pragma unroll
    for (int j = 0; j < 4; ++j) {
      const f32x4 z = MFMA(shi, X[2 * j + 1], MFMA(slo, X[2 * j], ZERO4));
#pragma unroll
      for (int r = 0; r < 4; ++r) s0 += gelu_tanh(z[r]) * ((r & 1) ? bfhi(ww[j][r >> 1]) : bf2f(ww[j][r >> 1]));
    }
    s0 += __uint_as_float(__builtin_amdgcn_mov_dpp(__float_as_uint(s0), 0x128, 0xf, 0xf, true));
    float* red = (float*)(smem + L_RED);
    if (c < 8) red[(((lane >> 4) & 1) * 8 + c) * 20 + 2 * w + (lane >> 5)] = s0;
    if (c == 0) { red[(2 * w) * 20 + 16 + (lane >> 4)] = lp0; red[(2 * w + 1) * 20 + 16 + (lane >> 4)] = lp1; }
    STAMP(17);
    BAR();
    if (w == 0 && lane < 16) {
      const f32x4* rp = (const f32x4*)(red + lane * 20);
      const f32x4 a = (rp[0] + rp[1]) + (rp[2] + rp[3]), b = rp[4];
      const float ld = rbf(rbf((a[0] + a[1]) + (a[2] + a[3])) + bf2f(b2[0]));
      out[bid * 16 + lane] = (u16)(pack2(rbf((b[0] + b[1]) + (b[2] + b[3])) + ld, 0.f) & 0xffffu);
      if (bid == 0 && lane == 0) __hip_atomic_store((gu64*)(ws + WS_EPOCH), tag, RLX_AGENT);
    }
  }
  STAMP(18);
  for (int i = 0; i < 100000 && __builtin_amdgcn_s_memrealtime() - t00 < 6000ull + 2 * (tk - t00); ++i) __builtin_amdgcn_s_sleep(1);
}

extern "C" void kernel_launch(void* const* d_in, const int* in_sizes, int n_in, void* d_out, int out_size,
                              void* d_ws, size_t ws_size, hipStream_t stream) {
  k_fused<<<256, 512, 0, stream>>>((const int*)d_in[0], (const char*)d_in[1], (const u16*)d_in[2], (const char*)d_in[3],
                                   (const u16*)d_in[4], (const u16*)d_in[5], (const u16*)d_in[6], (char*)d_ws, (u16*)d_out);
}
```

```cpp
#include <hip/hip_runtime.h>
#include <hip/hip_bf16.h>
#include <stdint.h>

typedef short bf16x8 __attribute__((ext_vector_type(8)));
typedef float f32x4 __attribute__((ext_vector_type(4)));
typedef float f32x2 __attribute__((ext_vector_type(2)));
typedef __bf16 bf16x2 __attribute__((ext_vector_type(2)));
typedef unsigned u32x4 __attribute__((ext_vector_type(4)));
typedef unsigned u32x2 __attribute__((ext_vector_type(2)));
typedef unsigned short u16;
typedef unsigned long long u64;
typedef __attribute__((address_space(1))) u64 gu64;
typedef __amdgpu_buffer_rsrc_t rsrc_t;

#define NSTAMP 23
constexpr int NB = 4096, NF = 100, ND = 128, NV = 1000, NH = 512;

constexpr int WS_EPOCH = 0;
constexpr int WS_F1   = 256;
constexpr int WS_F2   = 4096;
constexpr int WS_LX   = 32768;
constexpr int WS_WT   = 65536;
constexpr int WS_LIST = 131072;
constexpr int WS_X    = 1 << 20;
constexpr int WS_END  = WS_X + (16 << 20);
constexpr size_t WS_STAMP = 32u << 20;

#define STAMP(k)
#define STAMPL(k)
#define STAMPN(k)
#define STAMPNEND()
#define RSTAMP(k)

constexpr int L_P  = 0;
constexpr int L_WX = 1008 * 128;
constexpr int L_PX = L_WX + 2048;
constexpr int L_WB = L_PX + 1008 * 16;
constexpr int L_MISC = L_WB + 16384;
constexpr int L_TOTAL = L_MISC + 256;
constexpr int L_WT = L_PX;
constexpr int L_WT6 = L_WT + 21 * 1024;
constexpr int L_RED = L_WB + 8192;
static_assert(L_TOTAL == 163840, "lds");

#define MFMA(a, b, c) __builtin_amdgcn_mfma_f32_16x16x32_bf16(__builtin_bit_cast(bf16x8, a), __builtin_bit_cast(bf16x8, b), c, 0, 0, 0)
template <bool FIRST> __device__ __forceinline__ void mf(f32x4& d, const u32x4& a, const u32x4& b) {
  if constexpr (FIRST) asm volatile("v_mfma_f32_16x16x32_bf16 %0, %1, %2, 0" : "+v"(d) : "v"(a), "v"(b));
  else asm volatile("v_mfma_f32_16x16x32_bf16 %0, %1, %2, %0" : "+v"(d) : "v"(a), "v"(b));
}
template <int R> __device__ __forceinline__ void pin1(u32x4& x) {
  if constexpr (R == 16) asm volatile("" : "+{v[64:67]}"(x));
  if constexpr (R == 17) asm volatile("" : "+{v[68:71]}"(x));
  if constexpr (R == 18) asm volatile("" : "+{v[72:75]}"(x));
  if constexpr (R == 19) asm volatile("" : "+{v[76:79]}"(x));
  if constexpr (R == 20) asm volatile("" : "+{v[80:83]}"(x));
  if constexpr (R == 22) asm volatile("" : "+{v[88:91]}"(x));
  if constexpr (R == 21) asm volatile("" : "+{v[84:87]}"(x));
}
template <int OFF> __device__ __forceinline__ void dma16(unsigned vo, int lds, rsrc_t rs, int so) {
  asm volatile("s_mov_b32 m0, %1\n\ts_nop 0\n\tbuffer_load_dwordx4 %0, %2, %3 offen offset:%4 lds" :: "v"(vo), "s"(lds), "s"(rs), "s"(so), "i"(OFF) : "memory");
}
__device__ __forceinline__ void set_m0(int lds) { asm volatile("s_mov_b32 m0, %0\n\ts_nop 0" :: "s"(lds) : "memory"); }
template <int OFF> __device__ __forceinline__ void dma16n(unsigned vo, rsrc_t rs, int so) {
   asm volatile("buffer_load_dwordx4 %0, %1, %2 offen offset:%3 lds" :: "v"(vo), "s"(rs), "s"(so), "i"(OFF) : "memory");
}
template <class X> __device__ __forceinline__ void use(const X& x) { asm volatile("" :: "v"(x) : "memory"); }
template <int OFF> __device__ __forceinline__ void ld16(u32x4& x, unsigned vo, rsrc_t rs, int so) {
  asm volatile("buffer_load_dwordx4 %0, %1, %2, %3 offen offset:%4" : "=v"(x) : "v"(vo), "s"(rs), "s"(so), "i"(OFF));
}
template <int N> __device__ __forceinline__ void wait4(u32x4 (&x)[4]) {
  asm volatile("s_waitcnt vmcnt(%4)" : "+v"(x[0]), "+v"(x[1]), "+v"(x[2]), "+v"(x[3]) : "i"(N));
}
template <class X> __device__ __forceinline__ void opaque(X& x) { asm volatile("" : "+v"(x)); }
#define RESERVE(x) asm volatile("" : "=v"(x))
template <int I> __device__ __forceinline__ void mfma_acc(f32x4& c, const u32x4& a, const u32x4& b) {
  if constexpr (I == 0) asm volatile("v_mfma_f32_16x16x32_bf16 %0, %1, %2, %0" : "+{v[192:195]}"(c) : "v"(a), "v"(b));
  if constexpr (I == 1) asm volatile("v_mfma_f32_16x16x32_bf16 %0, %1, %2, %0" : "+{v[196:199]}"(c) : "v"(a), "v"(b));
  if constexpr (I == 2) asm volatile("v_mfma_f32_16x16x32_bf16 %0, %1, %2, %0" : "+{v[200:203]}"(c) : "v"(a), "v"(b));
  if constexpr (I == 3) asm volatile("v_mfma_f32_16x16x32_bf16 %0, %1, %2, %0" : "+{v[204:207]}"(c) : "v"(a), "v"(b));
  if constexpr (I == 4) asm volatile("v_mfma_f32_16x16x32_bf16 %0, %1, %2, %0" : "+{v[208:211]}"(c) : "v"(a), "v"(b));
  if constexpr (I == 5) asm volatile("v_mfma_f32_16x16x32_bf16 %0, %1, %2, %0" : "+{v[212:215]}"(c) : "v"(a), "v"(b));
  if constexpr (I == 6) asm volatile("v_mfma_f32_16x16x32_bf16 %0, %1, %2, %0" : "+{v[216:219]}"(c) : "v"(a), "v"(b));
  if constexpr (I == 7) asm volatile("v_mfma_f32_16x16x32_bf16 %0, %1, %2, %0" : "+{v[220:223]}"(c) : "v"(a), "v"(b));
  if constexpr (I == 8) asm volatile("v_mfma_f32_16x16x32_bf16 %0, %1, %2, %0" : "+{v[224:227]}"(c) : "v"(a), "v"(b));
  if constexpr (I == 9) asm volatile("v_mfma_f32_16x16x32_bf16 %0, %1, %2, %0" : "+{v[228:231]}"(c) : "v"(a), "v"(b));
  if constexpr (I == 10) asm volatile("v_mfma_f32_16x16x32_bf16 %0, %1, %2, %0" : "+{v[232:235]}"(c) : "v"(a), "v"(b));
  if constexpr (I == 11) asm volatile("v_mfma_f32_16x16x32_bf16 %0, %1, %2, %0" : "+{v[236:239]}"(c) : "v"(a), "v"(b));
  if constexpr (I == 12) asm volatile("v_mfma_f32_16x16x32_bf16 %0, %1, %2, %0" : "+{v[240:243]}"(c) : "v"(a), "v"(b));
  if constexpr (I == 13) asm volatile("v_mfma_f32_16x16x32_bf16 %0, %1, %2, %0" : "+{v[244:247]}"(c) : "v"(a), "v"(b));
  if constexpr (I == 14) asm volatile("v_mfma_f32_16x16x32_bf16 %0, %1, %2, %0" : "+{v[248:251]}"(c) : "v"(a), "v"(b));
  if constexpr (I == 15) asm volatile("v_mfma_f32_16x16x32_bf16 %0, %1, %2, %0" : "+{v[252:255]}"(c) : "v"(a), "v"(b));
}
template <int M> __device__ __forceinline__ void pin4(u32x4 (&x)[4]) {
  if constexpr (M == 0) asm volatile("" : "+{v[64:67]}"(x[0]), "+{v[68:71]}"(x[1]), "+{v[72:75]}"(x[2]), "+{v[76:79]}"(x[3]));
  if constexpr (M == 1) asm volatile("" : "+{v[80:83]}"(x[0]), "+{v[84:87]}"(x[1]), "+{v[88:91]}"(x[2]), "+{v[92:95]}"(x[3]));
  if constexpr (M == 2) asm volatile("" : "+{v[96:99]}"(x[0]), "+{v[100:103]}"(x[1]), "+{v[104:107]}"(x[2]), "+{v[108:111]}"(x[3]));
  if constexpr (M == 3) asm volatile("" : "+{v[112:115]}"(x[0]), "+{v[116:119]}"(x[1]), "+{v[120:123]}"(x[2]), "+{v[124:127]}"(x[3]));
  if constexpr (M == 4) asm volatile("" : "+{v[128:131]}"(x[0]), "+{v[132:135]}"(x[1]), "+{v[136:139]}"(x[2]), "+{v[140:143]}"(x[3]));
  if constexpr (M == 5) asm volatile("" : "+{v[144:147]}"(x[0]), "+{v[148:151]}"(x[1]), "+{v[152:155]}"(x[2]), "+{v[156:159]}"(x[3]));
  if constexpr (M == 6) asm volatile("" : "+{v[160:163]}"(x[0]), "+{v[164:167]}"(x[1]), "+{v[168:171]}"(x[2]), "+{v[172:175]}"(x[3]));
  if constexpr (M == 7) asm volatile("" : "+{v[176:179]}"(x[0]), "+{v[180:183]}"(x[1]), "+{v[184:187]}"(x[2]), "+{v[188:191]}"(x[3]));
  if constexpr (M == 8) asm volatile("" : "+{v[192:195]}"(x[0]), "+{v[196:199]}"(x[1]), "+{v[200:203]}"(x[2]), "+{v[204:207]}"(x[3]));
  if constexpr (M == 9) asm volatile("" : "+{v[208:211]}"(x[0]), "+{v[212:215]}"(x[1]), "+{v[216:219]}"(x[2]), "+{v[220:223]}"(x[3]));
  if constexpr (M == 10) asm volatile("" : "+{v[224:227]}"(x[0]), "+{v[228:231]}"(x[1]), "+{v[232:235]}"(x[2]), "+{v[236:239]}"(x[3]));
  if constexpr (M == 11) asm volatile("" : "+{v[240:243]}"(x[0]), "+{v[244:247]}"(x[1]), "+{v[248:251]}"(x[2]), "+{v[252:255]}"(x[3]));
}
#define BAR() asm volatile("s_waitcnt lgkmcnt(0)\n\ts_barrier" ::: "memory")
#define SCHED() __builtin_amdgcn_sched_barrier(0)
#define DRAIN() asm volatile("s_waitcnt vmcnt(0)" ::: "memory")
#define RLX_AGENT __ATOMIC_RELAXED, __HIP_MEMORY_SCOPE_AGENT
constexpr int SC1 = 16;
#define BLD128(rs, vo, so) __builtin_bit_cast(u32x4, __builtin_amdgcn_raw_buffer_load_b128(rs, vo, so, 0))
#define WNT 0
#define WNTS ""
#define BLD128W(rs, vo, so) __builtin_bit_cast(u32x4, __builtin_amdgcn_raw_buffer_load_b128(rs, vo, so, WNT1))
#define BLD128C(rs, vo, so) __builtin_bit_cast(u32x4, __builtin_amdgcn_raw_buffer_load_b128(rs, vo, so, SC1))
#define BST128C(v, rs, vo, so) do { const u32x4 v_ = v; __builtin_amdgcn_raw_buffer_store_b128(v_, rs, vo, so, SC1); asm volatile("s_nop 1" :: "v"(v_)); } while (0)
#define ZERO4 ((f32x4){0.f, 0.f, 0.f, 0.f})

template <int I> struct IC { static constexpr int v = I; };
template <int B, int E, class F> __device__ __forceinline__ void sfor(F&& f) {
  if constexpr (B < E) { f(IC<B>{}); sfor<B + 1, E>(f); }
}
__device__ __forceinline__ rsrc_t mkrs(const void* p, unsigned bytes) {
  return __builtin_amdgcn_make_buffer_rsrc((void*)p, 0, bytes, 0x00020000);
}
__device__ __forceinline__ unsigned pack2(float a, float b) {
  f32x2 v = {a, b};
  return __builtin_bit_cast(unsigned, __builtin_convertvector(v, bf16x2));
}
__device__ __forceinline__ unsigned umin_(unsigned a, unsigned b) { return a < b ? a : b; }
__device__ __forceinline__ unsigned umax_(unsigned a, unsigned b) { return a > b ? a : b; }
__device__ __forceinline__ float bf2f(unsigned x16) { return __uint_as_float(x16 << 16); }
__device__ __forceinline__ float bfhi(unsigned x) { return __uint_as_float(x & 0xffff0000u); }
__device__ __forceinline__ float rbf(float x) { return __uint_as_float(pack2(x, 0.f) << 16); }
__device__ __forceinline__ int fresh_lane() {
  unsigned z;
  asm volatile("s_mov_b32 %0, 0" : "=s"(z));
  const int l = (int)__builtin_amdgcn_mbcnt_hi(~0u, __builtin_amdgcn_mbcnt_lo(~0u, z));
  __builtin_assume(l >= 0 && l < 64);
  return l;
}
__device__ __forceinline__ u32x4 make_sel(int c, int q) {
  u32x4 sel;
  const int e = c - 8 * (q & 1);
  const unsigned one = (e & 1) ? 0x3F800000u : 0x3F80u;
#pragma unroll
  for (int k = 0; k < 4; ++k) sel[k] = ((e >> 1) == k) ? one : 0u;
  asm volatile("s_nop 1" : "+v"(sel));
  return sel;
}
__device__ __forceinline__ int w_src(int lane, int w) {
  const int jp = lane & 3, sf = (lane >> 2) & 7, sq = (lane >> 5) + 2 * (w & 1), ss = w >> 1;
  return (sf * 128 + 32 * sq + 8 * ss + 2 * jp) * 1024;
}
__device__ __forceinline__ void stage_w(char* smem, int lane, int w, u32x4 r0, u32x4 r1) {
  const int jp = lane & 3, sf = (lane >> 2) & 7, sq = (lane >> 5) + 2 * (w & 1), ss = w >> 1;
  const int wdst = L_WB + (ss * 64 + sq * 16 + 2 * sf) * 16 + jp * 4;
#pragma unroll
  for (int h = 0; h < 8; ++h) {
    const unsigned d = __builtin_amdgcn_perm(r1[h >> 1], r0[h >> 1], (h & 1) ? 0x07060302u : 0x05040100u);
    *(unsigned*)(smem + wdst + (h & 3) * 4096 + (h >> 2) * 16) = d;
  }
}
__device__ __forceinline__ unsigned half16(unsigned x, int hi) {
  unsigned r;
  if (hi) asm("v_lshlrev_b32_sdwa %0, 4, %1 dst_sel:DWORD dst_unused:UNUSED_PAD src0_sel:DWORD src1_sel:WORD_1" : "=v"(r) : "v"(x));
  else asm("v_lshlrev_b32_sdwa %0, 4, %1 dst_sel:DWORD dst_unused:UNUSED_PAD src0_sel:DWORD src1_sel:WORD_0" : "=v"(r) : "v"(x));
  return r;
}
__device__ __forceinline__ int list_off(int lane) {
  const int c = lane & 15, q = lane >> 4;
  return ((q & 1) * 2 + (c >> 3)) * 256 + ((q >> 1) * 8 + (c & 7)) * 16;
}
struct QT { u32x4 t0, t2; };
__device__ __forceinline__ void qt_init(QT& t) { asm volatile("s_nop 7" : "=v"(t.t0), "=v"(t.t2)); }
#define CNDX(perm, o, own, oth, m) do { register u64 m_ asm("vcc") = m; unsigned o_ = o; \
  asm volatile("v_cndmask_b32_dpp %0, %2, %1, vcc quad_perm:" perm " row_mask:0xf bank_mask:0xf" : "+v"(o_) : "v"(own), "v"(oth), "s"(m_)); o = o_; } while (0)
#define CNDXI(perm, own, oth, m) do { register u64 m_ asm("vcc") = m; unsigned o_ = own; \
  asm volatile("v_cndmask_b32_dpp %0, %1, %0, vcc quad_perm:" perm " row_mask:0xf bank_mask:0xf" : "+v"(o_) : "v"(oth), "s"(m_)); own = o_; } while (0)
__device__ __forceinline__ void qt_step(u32x4 (&x)[4], QT& t, int n) {
  const int st = n >> 3, p = (n >> 2) & 1, d = n & 3;
  if (st == 0) {
    if (p == 0) CNDX("[1,0,3,2]", t.t0[d], x[0][d], x[1][d], 0x5555555555555555ull);
    else        CNDX("[1,0,3,2]", t.t2[d], x[2][d], x[3][d], 0x5555555555555555ull);
  } else if (st == 1) {
    if (p == 0) CNDXI("[1,0,3,2]", x[3][d], x[2][d], 0xAAAAAAAAAAAAAAAAull);
    else        CNDXI("[1,0,3,2]", x[1][d], x[0][d], 0xAAAAAAAAAAAAAAAAull);
  } else if (st == 2) {
    if (p == 0) CNDX("[2,3,0,1]", x[0][d], t.t0[d], t.t2[d], 0x3333333333333333ull);
    else        CNDX("[2,3,0,1]", x[2][d], x[1][d], x[3][d], 0x3333333333333333ull);
  } else {
    if (p == 0) CNDXI("[2,3,0,1]", t.t2[d], t.t0[d], 0xCCCCCCCCCCCCCCCCull);
    else        CNDXI("[2,3,0,1]", x[3][d], x[1][d], 0xCCCCCCCCCCCCCCCCull);
  }
  if (n == 31) { const u32x4 dead = x[1]; x[1] = x[2]; x[2] = t.t2; t.t2 = dead; asm volatile("s_nop 1"); SCHED(); }
}
__device__ __forceinline__ void qt_all(u32x4 (&x)[4], QT& t) {
#pragma unroll
  for (int n = 0; n < 32; ++n) qt_step(x, t, n);
}
__device__ __forceinline__ void wait_flags(const char* p, u64 tag) {
  gu64* f = (gu64*)(p + (fresh_lane() & 31) * 8);
#pragma unroll 1
  for (int spins = 0; spins < (1 << 16); ++spins) {
    const u64 v = __hip_atomic_load(f, RLX_AGENT);
    if (__all(v == tag)) break;
    __builtin_amdgcn_s_sleep(2);
  }
  __builtin_amdgcn_fence(__ATOMIC_ACQUIRE, "wavefront");
}
__device__ __forceinline__ float gelu_arg(float x) {
  return x * __builtin_fmaf(x * x, -2.f * 0.7978845608028654f * 0.044715f * 1.4426950408889634f, -2.f * 0.7978845608028654f * 1.4426950408889634f);
}
__device__ __forceinline__ void RESERVE4(f32x4& x) { asm volatile("" : "=v"(x)); }

__device__ __forceinline__ float row_sum(float x) {
  x += __uint_as_float(__builtin_amdgcn_mov_dpp(__float_as_uint(x), 0xB1, 0xf, 0xf, true));
  x += __uint_as_float(__builtin_amdgcn_mov_dpp(__float_as_uint(x), 0x4E, 0xf, 0xf, true));
  x += __uint_as_float(__builtin_amdgcn_mov_dpp(__float_as_uint(x), 0x141, 0xf, 0xf, true));
  x += __uint_as_float(__builtin_amdgcn_mov_dpp(__float_as_uint(x), 0x140, 0xf, 0xf, true));
  return x;
}

#define NOBLANK 0
#define TWM 7
#define TWK 2
#define TWE 9
#define CT 1
#define TWP 0
#define PU 10
#define ONOP 0
#define OTR 128
#define KCK 8
#define DS 6
#define LAT 16
#define KX 130
#define XS 4
#define KF 184
#define POP2 0
#define POG 0
#define POP 0
#define PYG 1
#define PYP 1
#define PYP2 0
#define ROT 1
#define TT 1
#define NPRE 1
#define NPOST 2
#define WNT1 0
#define E2CAP 18
#define PRIO 1
#define WTD 4
#define E2G 8
#define PREPW 0
#define NB1 6
#define WLK 5
#define P1 5
#ifndef NVI
#define NVI 30
#endif
#define UXLD 6
#define IXD 5
#define TPRE 3
#define TPRE0 1
#define GDEP 4

constexpr int dk(int f) { return KCK + 1 + DS * f; }
constexpr int rk(int f) { return dk(f < 24 ? (f | 3) : 24) + LAT + (f < 24 ? 0 : 8) + 2 * (f & 3); }
constexpr int xk(int i) { return KX + XS * i; }
constexpr int dat(int k) { for (int f = 0; f < 25; ++f) if (dk(f) == k) return f; return -1; }
constexpr int rat(int k) { for (int f = 0; f < 28; ++f) if (rk(f) == k) return f; return -1; }
constexpr int xat(int k) { for (int i = 0; i < 8; ++i) if (xk(i) == k) return i; return -1; }
constexpr int younger(int k, int f) {
  int n = 0;
  for (int e = f + 1; e < 25; ++e) n += dk(e) <= k;
  for (int i = 0; i < 8; ++i) n += xk(i) >= dk(f) && xk(i) <= k;
  return n;
}
static_assert(dk(16) > rk(3) + 4 && dk(20) > rk(7) + 4 && dk(24) > rk(11) + 4 && KCK < 4 * (18 - WTD) && DS >= 2, "slots");
static_assert(rk(7) < 116 && rk(11) < 128 && rk(15) < 140 && rk(19) < 156 && rk(23) < 176 && rk(27) < 200 && rk(16) >= 112, "fragments");
static_assert(xk(7) < KF - 4 && KF + 49 < 240, "final");

__global__ void __launch_bounds__(512, 2)
k_fused(const int* __restrict__ idx, const char* __restrict__ table, const u16* __restrict__ wp,
        const char* __restrict__ w1, const u16* __restrict__ b1, const u16* __restrict__ w2,
        const u16* __restrict__ b2, char* __restrict__ ws, u16* __restrict__ out) {
  __shared__ __attribute__((aligned(16))) char smem[L_TOTAL];
  const int w = __builtin_amdgcn_readfirstlane(threadIdx.x >> 6);
  const int bid = blockIdx.x, xl = bid & 7, rr = bid >> 3;
  const int g = xl * 8 + (rr & 7), phi = rr >> 3;
  const rsrc_t rs_w1 = mkrs(w1, NF * ND * NH * 2), rs_tab = mkrs(table, NV * ND * 2), rs_ws = mkrs(ws, WS_END);
  const rsrc_t rs_idx = mkrs(idx, NB * NF * 4);
  const int wsb = phi * 24 * 128 * 1024 + g * 16;
  const int lsb = WS_LIST + (phi * 3 * 256 + w * 32) * 256;
  asm volatile("" :: "s"(idx), "s"(table), "s"(wp), "s"(w1), "s"(b1), "s"(w2), "s"(b2), "s"(ws), "s"(out));
  RSTAMP(62); STAMP(0);
  u64 pc0; unsigned csz;
  asm volatile("s_getpc_b64 %0\n.Lcode_begin:\n\ts_mov_b32 %1, .Lcode_end-.Lcode_begin" : "=s"(pc0), "=s"(csz));
  const rsrc_t rs_code = mkrs((const void*)pc0, csz);

  const u64 tag = *(const __attribute__((address_space(4))) u64*)(ws + WS_EPOCH) * 6364136223846793005ull + 1442695040888963407ull;
  u32x4 T[8][4];
  const int tsb = w * 128 * 256, slice = L_P + w * 16384;
  unsigned dv[4];
  auto dma_t = [&](auto M, auto I) {
    constexpr int m = M.v, i = I.v, slot = m < 4 ? m : m - 3;
    dma16<i * 1024>(dv[i], slice + slot * 4096, rs_tab, tsb + m * 4096);
  };
  u32x4 pi[6];
  unsigned la, lb, wa, wb, tw = 0;
  {
    const int lane = fresh_lane();
    if (w == PREPW) {
      const int c7 = lane & 7, po = c7 * (NF * 4) + (lane >> 4) * 32 + (((lane >> 3) ^ (lane >> 2)) & 1) * 16;
      sfor<0, 6>([&](auto K) { ld16<(K.v & 1) * (8 * NF * 4) + (K.v >> 1) * 128>(pi[K.v], po, rs_idx, bid * 16 * NF * 4); });
      asm volatile("buffer_load_dword %0, %2, %3, %4 offen offset:384\n\tbuffer_load_dword %1, %2, %3, %4 offen offset:3584"
                   : "=&v"(la), "=&v"(lb) : "v"(c7 * (NF * 4) + ((lane >> 3) & 3) * 4), "s"(rs_idx), "s"(bid * 16 * NF * 4));
    }
    unsigned tt = 0;
    if (TT && w == 7) asm volatile("buffer_load_dword %0, %1, %2, %3 offen" : "=v"(tt) : "v"(lane * 128), "s"(rs_tab), "s"(rr * 8192));
    if (w == 6) asm volatile("buffer_load_dword %0, %1, %2, %3 offen nt" : "=v"(tt) : "v"(lane < 16 ? lane * 1024 : 0x7f000000), "s"(rs_w1), "s"(((96 + phi) * 128 + (rr & 7) * 16) * 1024 + g * 16));
    if (CT && w == 5) asm volatile("buffer_load_dword %0, %1, %2, %3 offen" : "=v"(tt) : "v"(lane * 128), "s"(rs_code), "s"((rr & 7) * 8192));
    const int wv = ROT ? (w + rr) & 7 : w;
    u32x4 wp0, wp1;
    asm volatile("buffer_load_dwordx4 %0, %2, %3, %4 offen" WNTS "\n\tbuffer_load_dwordx4 %1, %2, %3, %4 offen offset:1024" WNTS
                 : "=&v"(wp0), "=&v"(wp1) : "v"(w_src(lane, wv)), "s"(rs_w1), "s"(wsb));
    SCHED();
    if (w == 0 && lane < 2) *(u32x4*)(smem + L_MISC + lane * 16) = (u32x4){0u, 0u, 0u, 0u};
    {
      const int h = (lane & 15) ^ (lane >> 4);
      dv[0] = (lane >> 4) * 256 + (h ^ ((h & 4) << 1)) * 16;
      dv[1] = dv[0] ^ 192; dv[2] = dv[0] ^ 128; dv[3] = dv[0] ^ 64;
    }
    SCHED();
    if (w == PREPW) {
      const rsrc_t rs_wp = mkrs(wp, NF * NF * 2);
      const int id = bid * 23 + lane, tile = id >> 8, l = (id >> 2) & 63, r = id & 3, c = l & 15, q = l >> 4;
      int tj = (tile >= 1) + (tile >= 3) + (tile >= 6) + (tile >= 10) + (tile >= 15);
      int i = 16 * (tile - tj * (tj + 1) / 2) + 4 * q + r, j = 16 * tj + c;
      bool oka = true, okb = (i >> 4) != tj;
      if (tile >= 21) {
        tj = (tile - 21) * 4 + r; i = 96 + q; j = tj < 6 ? 16 * tj + c : 96 + (c >> 2);
        oka = tj < 6 || (tj == 6 && (c & 3) == 0); okb = tj < 6;
      }
      asm volatile("buffer_load_ushort %0, %2, %4, 0 offen\n\tbuffer_load_ushort %1, %3, %4, 0 offen"
                   : "=&v"(wa), "=&v"(wb) : "v"((oka && lane < 23) ? (i * NF + j) * 2 : 0x100000), "v"((okb && lane < 23) ? (j * NF + i) * 2 : 0x100000), "s"(rs_wp));
      asm volatile("s_waitcnt vmcnt(2)" ::: "memory");
    } else asm volatile("s_waitcnt vmcnt(0)" ::: "memory");
    asm volatile("" : "+v"(wp0), "+v"(wp1) : "v"(tt) : "memory");
    stage_w(smem, lane, wv, wp0, wp1);
    SCHED();
    if (w == PREPW) {
      asm volatile("" : "+v"(la), "+v"(lb), "+v"(pi[0]), "+v"(pi[1]), "+v"(pi[2]), "+v"(pi[3]), "+v"(pi[4]), "+v"(pi[5]));
      const int jb = (((lane >> 3) ^ (lane >> 2)) & 1) * 4;
      const unsigned k0 = jb * 0x10001u + 0x10000u, k1 = k0 + 0x20002u, sh = (lane & 1) * 16;
      const bool sw = lane & 2;
#pragma unroll
      for (int k = 0; k < 3; ++k) {
        u32x4 o;
#pragma unroll
        for (int tp = 0; tp < 2; ++tp) {
          const u32x4 a = pi[2 * k + tp];
          const unsigned p0 = ((a[0] | (a[1] << 16)) << 3) + k0, p1 = ((a[2] | (a[3] << 16)) << 3) + k1;
          const unsigned r0 = __builtin_amdgcn_alignbit(p0, p0, sh), r1 = __builtin_amdgcn_alignbit(p1, p1, sh);
          o[2 * tp] = sw ? r1 : r0; o[2 * tp + 1] = sw ? r0 : r1;
        }
        BST128C(o, rs_ws, (lane >> 4) * 65536 + (lane & 15) * 16, WS_LIST + k * 4 * 65536 + bid * 256);
      }
      __builtin_amdgcn_raw_buffer_store_b32(la | (lb << 16), rs_ws, lane < 32 ? (lane >> 3) * 8192 + (lane & 7) * 16 : 0x7f000000,
                                            WS_LX + (bid >> 5) * 1024 + (bid & 7) * 128 + ((bid >> 3) & 3) * 4, SC1);
      asm volatile("s_waitcnt vmcnt(4)" : "+v"(wa), "+v"(wb));
      __builtin_amdgcn_raw_buffer_store_b32(__float_as_uint(bf2f(wa) + bf2f(wb)), rs_ws, lane < 23 ? lane * 4 : 0x7f000000, WS_WT + bid * 92, SC1);
    }
    SCHED();
    sfor<0, 4 * NPRE>([&](auto I) { dma_t(IC<(I.v >> 2)>{}, IC<(I.v & 3)>{}); });
  }
  STAMP(1);
  BAR();
  STAMP(2);
  if (P1 && w >= 4) __builtin_amdgcn_s_setprio(1);
  auto touch_l = [&](int ph) -> unsigned {
    const int lane = fresh_lane();
    return __builtin_amdgcn_raw_buffer_load_b32(rs_ws, lane < 8 ? ((rr & 7) * 8 + lane) * 128 : 0x7f000000, lsb + ph * 65536, SC1);
  };

  u32x4 ix[IXD];
  auto load_ix = [&](int ph, int k0, int k1) {
    const int lsrc = list_off(fresh_lane());
#pragma unroll
    for (int k = k0; k < k1; ++k) ix[k] = BLD128C(rs_ws, lsrc + (k & 3) * 1024, lsb + ph * 65536 + (k >> 2) * 4096);
  };
  f32x4 acc[16];
  {
    const int lane = fresh_lane(), c = lane & 15, q = lane >> 4;
    int pw = L_P + (128 * w + 4 * q) * 128 + c * 8;
    u32x4 Wf[4][4];
    auto read_w = [&](int h) {
#pragma unroll
      for (int k = 0; k < 8; ++k) Wf[2 * h + (k & 1)][k >> 1] = *(const u32x4*)(smem + L_WB + ((2 * h + (k & 1)) * 4 + (k >> 1)) * 1024 + lane * 16);
    };
    read_w(0);
    SCHED();
    sfor<4 * NPRE, 4 * NPOST>([&](auto I) { dma_t(IC<(I.v >> 2)>{}, IC<(I.v & 3)>{}); });
    SCHED();
    const int toff = ((lane >> 2) & 3) * 1024 + (lane >> 4) * 64 + (lane & 3) * 16;
    int ra[4];
    ra[0] = slice + c * 256 + (((q ^ ((q & 1) << 1)) << 2) ^ c) * 16;
    ra[1] = ra[0] ^ 16; ra[2] = ra[0] ^ 32; ra[3] = ra[0] ^ 48;
    asm volatile("" : "+v"(ra[0]), "+v"(ra[1]), "+v"(ra[2]), "+v"(ra[3]), "+v"(pw));
    auto read_t = [&](int m, int s) { T[m][s] = *(const u32x4*)(smem + ra[s] + (m < 4 ? m : m - 3) * 4096); };
    auto load_t = [&](auto M, auto I) {
      ld16<I.v * 256>(T[M.v][I.v], toff, rs_tab, tsb + M.v * 4096);
    };
    asm volatile("s_waitcnt vmcnt(%0)" :: "i"(4 * NPOST - 4) : "memory");
#pragma unroll
    for (int s = 0; s < 4; ++s) read_t(0, s);
    SCHED();
    read_w(1);
    if (w == PREPW && lane == 0) __hip_atomic_store((gu64*)(ws + WS_F1 + bid * 8), tag, RLX_AGENT);
    u64 fl;
    u32x2 bz;
    u32x4 xr0, xr1;
    QT qt; qt_init(qt);
    f32x4 D[2][2];
    RESERVE(D[0][0]); RESERVE(D[0][1]); RESERVE(D[1][0]); RESERVE(D[1][1]);
    u32x2 o[4];
    SCHED();
    auto block = [&](auto M) {
      constexpr int m = M.v;
      if constexpr (m == P1) __builtin_amdgcn_s_setprio(0);
      sfor<0, 16>([&](auto K) {
        constexpr int k = K.v, h = k >> 3, s = (k >> 1) & 3, t = k & 1;
        if constexpr (m == 8 && k == 0) asm volatile("s_nop 7");
        if constexpr ((m == 3 || m == 6) && k == 0)
          wait4<(m == 3 ? 4 : 0)>(T[m + 1]);
        if constexpr (m < 8) mf<s == 0>(D[h][t], T[m][s], Wf[2 * h + t][s]);
        SCHED();
        if constexpr (m == 3 || m == 6) { qt_step(T[m + 1], qt, 2 * k); qt_step(T[m + 1], qt, 2 * k + 1); }
        if constexpr (m == 0 && NPOST == 2) {
          if constexpr (k & 1) dma_t(IC<2 + (k >> 3)>{}, IC<((k >> 1) & 3)>{});
        } else if constexpr ((k & 3) == 1 && m < 5) {
          if constexpr (m == 1 || m == 4) load_t(IC<m + 3>{}, IC<(k >> 2)>{}); else dma_t(IC<m + 3>{}, IC<(k >> 2)>{});
        }
        if constexpr (m != 3 && m < 6 && k >= 8 && k < 12) {
          if constexpr (k == 8) {
            constexpr int n = (m == 0 && NPOST == 2) ? 4 : m < 5 ? 6 : 4;
            if (m + 1 < NPOST && w == PREPW) asm volatile("s_waitcnt vmcnt(%0)" :: "i"(n + 1) : "memory");
            else asm volatile("s_waitcnt vmcnt(%0)" :: "i"(n) : "memory");
          }
          read_t(m + 1, k - 8);
        }
        if constexpr ((m == 1 || m == 2) && k == 15) asm volatile("s_waitcnt lgkmcnt(0)" ::: "memory");
        if constexpr (m == 6 && k == 1) {
          fl = __hip_atomic_load((gu64*)(ws + WS_F1 + w * 256 + (fresh_lane() & 31) * 8), RLX_AGENT);
          bz = __builtin_amdgcn_raw_buffer_load_b64(mkrs(b1, phi == 0 ? NH * 2 : 0), ((fresh_lane() >> 4) & 1) * 8, g * 16, 0);
          if (w == 7) {
            const int l2 = fresh_lane();
            xr0 = BLD128W(rs_w1, l2 * 2048, (96 + phi) * 128 * 1024 + g * 16);
            xr1 = BLD128W(rs_w1, l2 * 2048 + 1024, (96 + phi) * 128 * 1024 + g * 16);
          }
        }
        if constexpr (m == 7 && k == 0) {
          if (!__all(fl == tag)) wait_flags(ws + WS_F1 + w * 256, tag);
          __builtin_amdgcn_fence(__ATOMIC_ACQUIRE, "wavefront");
          load_ix(0, 0, IXD);
        }
        if constexpr (m == TWM && k == TWK) {
          const int l2 = fresh_lane();
          tw = __builtin_amdgcn_raw_buffer_load_b32(rs_w1, l2 < 32 ? (1024 + (l2 >> 4) * 1024 + ((rr & 7) * 8 + w) * 16 + (l2 & 15)) * 1024 : 0x7f000000, wsb, TWP);
        }
        if constexpr (m > 0 && k >= 4 && k < 8) {
          constexpr int r = k - 4;
          o[r][1] = pack2(D[1][0][r], D[1][1][r]);
          *(u32x2*)(smem + pw + (m - 1) * 2048 + r * 128) = o[r];
        }
        if constexpr (m < 8 && k >= 12) o[k - 12][0] = pack2(D[0][0][k - 12], D[0][1][k - 12]);
        SCHED();
      });
    };
    sfor<0, 8>(block);
    if (w < 7) block(IC<8>{});
    else {
      const int l2 = fresh_lane(), xd = L_WX + (((l2 >> 2) & 3) * 4 + (l2 >> 4)) * 128 + (l2 & 3) * 4;
#pragma unroll
      for (int h = 0; h < 8; ++h) {
        const unsigned d = __builtin_amdgcn_perm(xr1[h >> 1], xr0[h >> 1], (h & 1) ? 0x07060302u : 0x05040100u);
        *(unsigned*)(smem + xd + h * 16) = d;
      }
    }
    const f32x4 a0 = {bf2f(bz[0]), bfhi(bz[0]), bf2f(bz[1]), bfhi(bz[1])};
#pragma unroll
    for (int i = 0; i < 16; ++i) acc[i] = a0;
  }
  STAMP(3);
  BAR();
  STAMP(4);

  auto blank = [&](int k0, int k1) {
    u32x4 ones = {~0u, ~0u, ~0u, ~0u};
    opaque(ones);
    const int l16 = fresh_lane() * 16;
#pragma unroll
    for (int k = k0; k < k1; ++k) if (!NOBLANK) BST128C(ones, rs_ws, l16 + (k & 3) * 1024, WS_X + bid * 65536 + w * 16384 + (k >> 2) * 4096);
  };
  sfor<0, 2>([&](auto PH) {
    constexpr int ph = PH.v;
    sfor<0, 8>([&](auto M) { pin4<M.v>(T[M.v]); });
    u32x4 lx;
    {
      const int lane = fresh_lane(), c = lane & 15, q = lane >> 4;
      const int lsrc = list_off(lane);
      u32x4 wr0, wr1;
      auto load_w = [&](int v) {
        const int wsrc = w_src(fresh_lane(), v);
        wr0 = BLD128W(rs_w1, wsrc, wsb + (ph + 1) * 8 * 128 * 1024); wr1 = BLD128W(rs_w1, wsrc + 1024, wsb + (ph + 1) * 8 * 128 * 1024);
      };
      const u32x4 sel = make_sel(c, q);
      constexpr int DEP = GDEP;
      u32x4 gv[DEP];
      auto gaddr = [&](int n) -> unsigned {
        return half16(ix[(n >> 3) % IXD][(n >> 1) & 3], n & 1);
      };
      u32x4 Wx[4];
      f32x4 dx[2];
      int xw = L_PX + (128 * w + c) * 16 + (q & 1) * 8;
      if constexpr (ph == 0) {
#pragma unroll
        for (int s = 0; s < 4; ++s) Wx[s] = *(const u32x4*)(smem + L_WX + (s * 4 + q) * 128 + (c & 7) * 16);
        RESERVE(dx[0]); RESERVE(dx[1]);
        asm volatile("" : "+v"(xw));
      }
#pragma unroll
      for (int n = 0; n < DEP; ++n) gv[n] = *(const u32x4*)(smem + L_P + gaddr(n));
      sfor<0, 64>([&](auto N) {
        constexpr int n = N.v;
        if constexpr (ph == 0 && n < 32) mf<(n & 3) == 0>(dx[(n >> 2) & 1], Wx[n & 3], T[n >> 2][n & 3]);
        if constexpr (ph == 0 && n >= 4 && n < 36 && (n & 3) == 3) {
          constexpr int m = (n >> 2) - 1;
          SCHED();
          if (m < 7 || w < 7) *(u32x2*)(smem + xw + m * 256) = (u32x2){pack2(dx[m & 1][0], dx[m & 1][1]), pack2(dx[m & 1][2], dx[m & 1][3])};
          SCHED();
        }
        if constexpr (ph == 0 && (n & 3) == 0) acc[n >> 2] = MFMA(sel, gv[n % DEP], acc[n >> 2]); else mfma_acc<(n >> 2)>(acc[n >> 2], sel, gv[n % DEP]);
        SCHED();
        if (n + DEP < 64) gv[n % DEP] = *(const u32x4*)(smem + L_P + gaddr(n + DEP));
        if (((n + DEP) & 7) == 7) {
          const int k = ((n + DEP) >> 3) + IXD;
          if (k < 8) ix[k % IXD] = BLD128C(rs_ws, lsrc + (k & 3) * 1024, lsb + ph * 65536 + (k >> 2) * 4096);
          if (k == WLK && w < 4) load_w(w);
          if (ph == 0 && k == TWE) asm volatile("" :: "v"(tw));
          if (k == WLK + 3 && w < 4) { stage_w(smem, fresh_lane(), w, wr0, wr1); load_w(w + 4); }
          if (k == 10 && ph == 0 && w >= 4) lx = BLD128C(rs_ws, fresh_lane() * 16, WS_LX + phi * 8192 + w * 1024);
        }
        SCHED();
      });
      if (w < 4) {
        const int l2 = fresh_lane();
        u32x4 wt[4];
        if constexpr (ph == 1) {
#pragma unroll
          for (int k = 0; k < 4; ++k) wt[k] = BLD128C(rs_ws, l2 * 16 + k * 1024, WS_WT + w * 4096);
        }
        stage_w(smem, l2, w + 4, wr0, wr1);
        if constexpr (ph == 0) blank(0, NB1);
        else {
#pragma unroll
          for (int k = 0; k < 4; ++k) if (k < 3 || w < 3) *(u32x4*)(smem + L_WT + w * 4096 + k * 1024 + l2 * 16) = wt[k];
          DRAIN();
        }
      }
    }
    STAMPL(5);
    BAR();
    if (ph == 1 && w == 0 && fresh_lane() == 0) __hip_atomic_store((gu64*)(ws + WS_F2 + bid * 8), tag, RLX_AGENT);
    STAMPL(6);
    auto extra_gather = [&]() {
      const int lane = fresh_lane(), c = lane & 15, q = lane >> 4;
      u32x4 gv[8];
#pragma unroll
      for (int n = 0; n < 8; ++n) gv[n] = *(const u32x4*)(smem + L_PX + half16(lx[n >> 1], n & 1));
      SCHED();
      const u32x4 sel = make_sel(c, q);
      u32x4 slo = q < 2 ? sel : (u32x4){0, 0, 0, 0}, shi = q < 2 ? (u32x4){0, 0, 0, 0} : sel;
      asm volatile("s_nop 1" : "+v"(slo), "+v"(shi));
      SCHED();
      sfor<0, 8>([&](auto N) {
        constexpr int n = N.v, a = 2 * (n & 6) + (n & 1);
        mfma_acc<a>(acc[a], slo, gv[n]);
        mfma_acc<a + 2>(acc[a + 2], shi, gv[n]);
      });
    };
    if constexpr (ph == 0) { if (w >= 4) extra_gather(); }
    SCHED();
    sfor<0, 8>([&](auto M) { pin4<M.v>(T[M.v]); });
    {
      const int lane = fresh_lane(), c = lane & 15, q = lane >> 4;
      int pw = L_P + (128 * w + 4 * q) * 128 + c * 8, wb = L_WB + lane * 16, eo;
      asm volatile("" : "+v"(pw), "+v"(wb));
      u32x4 Wf[2][4];
#pragma unroll
      for (int t = 0; t < 2; ++t)
#pragma unroll
        for (int s = 0; s < 4; ++s) Wf[t][s] = *(const u32x4*)(smem + wb + (t * 4 + s) * 1024);
      f32x4 D[2][2];
      RESERVE(D[0][0]); RESERVE(D[0][1]); RESERVE(D[1][0]); RESERVE(D[1][1]);
      unsigned tl, tc, pk[4];
      auto um = [](int u) { return ph ? (u >> 2) * 2 + (u & 1) : (u & 7); };
      auto uh = [](int u) { return ph ? ((u >> 1) ^ (u >> 2)) & 1 : (u >> 3); };
      sfor<0, 18>([&](auto U) {
        constexpr int u = U.v, m = um(u), h = uh(u);
        sfor<0, 8>([&](auto J) {
          constexpr int j = J.v, s = j >> 1, t = j & 1;
          if constexpr (u < 16) {
            mf<s == 0>(D[u & 1][t], T[m][s], Wf[t][s]);
            SCHED();
            if constexpr (u < 15 && uh(u + 1) != h) Wf[t][s] = *(const u32x4*)(smem + wb + (((h ^ 1) * 2 + t) * 4 + s) * 1024);
          }
          if constexpr (u >= 2 && j < 2) {
            *(unsigned*)(smem + pw + j * 256) = pk[2 * j];
            *(unsigned*)(smem + pw + j * 256 + 128) = pk[2 * j + 1];
          }
          if constexpr (u >= 2 && u < 17 && j == 2) { pw += (um(u - 1) - um(u - 2)) * 2048 + (uh(u - 1) - uh(u - 2)) * 4; asm volatile("" : "+v"(pw)); }
          if constexpr (u == 1 && j == 3) asm volatile("s_nop 2");
          if constexpr (u == 16 && j == 3) asm volatile("s_nop 3");
          if constexpr (u >= 1 && u < 17 && j >= 4) pk[j - 4] = pack2(D[(u - 1) & 1][0][j - 4], D[(u - 1) & 1][1][j - 4]);
          if constexpr (u == 2 && j == 3) tl = touch_l(ph + 1);
          if constexpr (ph == 1 && u == PU && j == 0) { if (w >= 4) __builtin_amdgcn_s_setprio(1); }
          if constexpr (CT >= 2 && u == 1 && j == 5) {
            const int l2 = fresh_lane();
            tc = __builtin_amdgcn_raw_buffer_load_b32(rs_code, l2 < 8 ? l2 * 128 : 0x7f000000, ((rr & 7) * 8 + w) * 1024, 0);
          }
          if constexpr (ph == 1) {
            if constexpr (u == 4 && j < 7) {
              if constexpr (j == 0) { eo = ((wb - L_WB) >> 2) & 48; asm volatile("" : "+v"(eo)); }
              if constexpr (j < 6) T[j >> 2][j & 3] = BLD128(rs_idx, eo + j * 64, (bid * 16 + w * 2) * NF * 4);
              else {
                T[1][2][0] = __builtin_amdgcn_raw_buffer_load_b32(rs_idx, (eo >> 2) + 384, (bid * 16 + w * 2) * NF * 4, 0);
                eo = (((wb - L_WB) >> 2) & 192) + ((wb - L_WB) & 48); asm volatile("" : "+v"(eo));
              }
            }
            if constexpr ((u == 7 || u == 8 || u == 11 || u == 12 || u == 15 || u == 16) && (j & 1) == 1) {
              constexpr int b = (u - 6) >> 1;
              T[b + 2][s] = BLD128(rs_tab, T[b >> 2][b & 3][s] * 256 + eo, 0);
            }
            if constexpr (u == 17 && j == 0) T[1][3] = BLD128(rs_tab, T[1][2][0] * 256 + eo, 0);
            if constexpr (u == 10 && j == 3) T[0][0] = BLD128C(rs_ws, wb - L_WB, WS_WT + (15 + w) * 1024);
            if constexpr (u == 14 && j == 3) {
              const u32x2 f = __builtin_amdgcn_raw_buffer_load_b64(rs_ws, ((wb - L_WB) >> 1) & 255, WS_F2 + w * 256, SC1);
              T[0][3][0] = f[0]; T[0][3][1] = f[1];
            }
          }
          if constexpr (u == 12 && j == 3) { asm volatile("" :: "v"(tl)); if constexpr (CT >= 2) asm volatile("" :: "v"(tc)); load_ix(ph + 1, 0, 1); }
          if constexpr (u == 15 && j == 7) {
            if constexpr (ph == 0) { if (w < 4) lx = BLD128C(rs_ws, wb - L_WB, WS_LX + phi * 8192 + w * 1024); }
            load_ix(ph + 1, 1, IXD);
          }
          SCHED();
        });
      });
    }
    if constexpr (ph == 0) { if (w < 4) { blank(NB1, 16); extra_gather(); } }
    STAMPL(7);
    if constexpr (ph == 0) { BAR(); STAMPL(8); }
  });

  u32x4 E[7][4], E2[7][4];
  u32x4 vi[7];
  u32x2 ww[4];
  QT qt;
#pragma unroll
  for (int t = 0; t < 6; ++t)
#pragma unroll
    for (int i = 0; i < 4; ++i) E[t][i] = T[t + 2][i];
  E[6][0] = T[1][3];
  auto gather3 = [&](auto OLD) __attribute__((always_inline)) {
    constexpr bool old = OLD.v;
    const int lane = fresh_lane(), c = lane & 15, q = lane >> 4;
    const int lsrc = list_off(lane);
    u32x4 jx[8];
#pragma unroll
    for (int k = 0; k < IXD; ++k) jx[k] = ix[k];
#pragma unroll
    for (int k = IXD; k < 8; ++k) jx[k] = BLD128C(rs_ws, lsrc + (k & 3) * 1024, lsb + 2 * 65536 + (k >> 2) * 4096);
    const u32x4 sel = make_sel(c, q);
    int xo = ((q >> 1) * 2 + (c >> 3)) * 65536 + ((q & 1) * 8 + (c & 7)) * 16, wo = L_WT + (15 + w) * 1024 + lane * 16;
    const int xb = WS_X + (w * 32 * 256 + g * 4 + phi) * 256;
    asm volatile("" : "+v"(xo), "+v"(wo));
    if (!__all((((u64)T[0][3][1] << 32) | T[0][3][0]) == tag)) wait_flags(ws + WS_F2 + w * 256, tag);
    qt_init(qt);
    SCHED();
    if constexpr (old) sfor<0, OTR>([&](auto K) {
      if constexpr ((K.v & 31) == 0) pin4<(K.v >> 5) + 2>(E[K.v >> 5]);
      qt_step(E[K.v >> 5], qt, K.v & 31);
      if constexpr (ONOP && (K.v & 1)) asm volatile("s_nop %0" :: "i"(ONOP - 1));
      SCHED();
    });
    STAMP(22);
    BAR();
    STAMP(12);
    __builtin_amdgcn_s_setprio(old ? POG : PYG);
    constexpr int DEP = GDEP;
    u32x4 gv[DEP];
    auto gaddr = [&](int n) -> unsigned { return half16(jx[n >> 3][(n >> 1) & 3], n & 1); };
#pragma unroll
    for (int n = 0; n < DEP; ++n) gv[n] = *(const u32x4*)(smem + L_P + gaddr(n));
    *(u32x4*)(smem + wo) = T[0][0];
    SCHED();
    sfor<0, 68>([&](auto N) {
      constexpr int n = N.v;
      if constexpr (n < 64) mfma_acc<(n >> 2)>(acc[n >> 2], sel, gv[n % DEP]);
      SCHED();
      if constexpr (n + DEP < 64) gv[n % DEP] = *(const u32x4*)(smem + L_P + gaddr(n + DEP));
      if constexpr (n + DEP == 64) {
        asm volatile("s_mov_b64 exec, 1\n\tds_add_u32 %0, %1\n\ts_mov_b64 exec, -1" :: "v"(L_MISC), "v"(1) : "memory");
      }
      if constexpr (n == 64) asm volatile("s_nop 5" : "+{v[92:95]}"(E[6][0]));
      if constexpr (n == NVI) {
#pragma unroll
        for (int t = 0; t < 6; ++t) vi[t] = BLD128(rs_idx, (lane & 48) + t * 64, (bid * 16 + w * 2 + 1) * NF * 4);
        vi[6][0] = __builtin_amdgcn_raw_buffer_load_b32(rs_idx, (lane >> 4) * 4 + 384, (bid * 16 + w * 2 + 1) * NF * 4, 0);
      }
      if constexpr (n == NVI + 10) {
#pragma unroll
        for (int j = 0; j < 4; ++j) ww[j] = __builtin_amdgcn_raw_buffer_load_b64(mkrs(w2, NH * 2), (lane >> 5) * 16 + (lane & 8) + j * 32, w * 128, 0);
      }
      if constexpr (n >= 11 && (n & 7) == 3) {
        constexpr int ch = (n >> 3) - 1;
        const f32x4 a = acc[2 * ch], b = acc[2 * ch + 1];
        BST128C(((u32x4){pack2(a[0], a[1]), pack2(a[2], a[3]), pack2(b[0], b[1]), pack2(b[2], b[3])}), rs_ws, xo, xb + ch * 4 * 65536);
      }
      constexpr int k0 = old ? OTR : 0, kn = (192 - k0) / 64;
      if constexpr (n < 64) sfor<k0 + kn * n, k0 + kn * n + kn>([&](auto K) { if constexpr ((K.v & 31) == 0) pin4<(K.v >> 5) + 2>(E[K.v >> 5]); qt_step(E[K.v >> 5], qt, K.v & 31); });
      SCHED();
    });
  };
  u32x4 X[8];
  auto pnn = [&](u32x4 (&Ee)[7][4], auto first, auto hook) __attribute__((always_inline)) -> float {
    int wa = L_WT + fresh_lane() * 16;
    asm volatile("" : "+v"(wa));
    f32x4 sm = ZERO4, D[3], wt[WTD];
    RESERVE(D[0]); RESERVE(D[1]); RESERVE(D[2]);
    sfor<0, WTD - 2>([&](auto J) { wt[J.v] = *(const f32x4*)(smem + wa + J.v * 1024); });
    sfor<0, 30>([&](auto U) {
      constexpr int u = U.v;
      constexpr int tj = u < 1 ? 0 : u < 3 ? 1 : u < 6 ? 2 : u < 10 ? 3 : u < 15 ? 4 : u < 21 ? 5 : 6, ti = u - tj * (tj + 1) / 2;
      if constexpr (u >= 1 && u + WTD - 3 < 23) wt[(u + WTD - 3) % WTD] = *(const f32x4*)(smem + wa + (u + WTD - 3) * 1024);
      sfor<0, 4>([&](auto S) {
        constexpr int s = S.v, k = u * 4 + s;
        if constexpr (u < 21) mf<s == 0>(D[u % 3], Ee[ti][s], Ee[tj][s]);
        else if constexpr (u < 28) mf<s == 0>(D[u % 3], Ee[6][s], Ee[ti][s]);
        else if constexpr (s == 0) asm volatile("s_nop 3");
        SCHED();
        if constexpr (first.v && k >= 64 && k < 80) {
          constexpr int j = 3 - ((k - 64) >> 2), d = k & 3;
          Ee[6][j][d] = __builtin_amdgcn_mov_dpp(Ee[6][0][d], j * 0x55, 0xf, 0xf, true);
        }
        if constexpr (u >= 2 && u - 2 < 21) { sm[s] += D[(u - 2) % 3][s] * wt[(u - 2) % WTD][s]; asm volatile("" : "+v"(sm[s])); }
        if constexpr (u - 2 >= 21 && s == 3) { sm[0] += D[(u - 2) % 3][0] * wt[(21 + ((u - 2 - 21) >> 2)) % WTD][(u - 2 - 21) & 3]; asm volatile("" : "+v"(sm[0])); }
        hook(IC<k>{});
        SCHED();
      });
    });
    return row_sum((sm[0] + sm[1]) + (sm[2] + sm[3]));
  };
  auto load_x = [&]() {
    const int lane = fresh_lane();
#pragma unroll
    for (int k = 0; k < 8; ++k) X[k] = BLD128C(rs_ws, lane * 16 + (k & 3) * 1024, WS_X + (bid * 256 + w * 32) * 256 + (k >> 2) * 4096);
  };
  auto rest = [&](auto OLD) __attribute__((always_inline)) {
    constexpr bool old = OLD.v;
    __builtin_amdgcn_s_setprio(old ? POP : PYP);
    STAMP(13);
    const rsrc_t rs_t2 = mkrs(table - 3072, NV * ND * 2 + 4096);
    int eo[4], er[4];
    auto dma_e = [&](auto F) {
      constexpr int f = F.v;
      if constexpr (f >= 16 && (f & 3) == 0) { use(E2[(f - 16) >> 2][0]); use(E2[(f - 16) >> 2][1]); use(E2[(f - 16) >> 2][2]); use(E2[(f - 16) >> 2][3]); }
      if constexpr ((f & 3) == 0) set_m0(slice + (f & 12) * 1024);
      dma16n<(f & 3) * 1024>(vi[f >> 2][f & 3] * 256 + eo[f & 3], rs_t2, 3072 - 1024 * (f & 3));
    };
    auto read_e = [&](auto F) { constexpr int f = F.v; E2[f >> 2][f & 3] = *(const u32x4*)(smem + er[f & 3] + (f & 12) * 1024); };
    int xl16;
    u32x4 slo, shi;
    f32x4 z[2];
    float s0 = 0.f, ya, yb;
    auto side = [&](auto K) {
      constexpr int k = K.v;
      if constexpr (k == KCK - 4) {
        const int lane = fresh_lane(), c = lane & 15, q = lane >> 4;
        eo[0] = (c ^ (4 * q)) * 16;
        er[0] = slice + ((c & 3) * 4 + (c >> 2)) * 256 + (((q ^ ((q & 1) << 1)) << 2) ^ c) * 16;
#pragma unroll
        for (int i = 1; i < 4; ++i) { eo[i] = eo[0] ^ (16 * i); er[i] = er[0] ^ (16 * i); }
#pragma unroll
        for (int i = 0; i < 4; ++i) { opaque(eo[i]); opaque(er[i]); }
        xl16 = lane * 16; opaque(xl16);
      }
      if constexpr (k == KCK) {
#pragma unroll 1
        for (int spins = 0; spins < (1 << 20); ++spins) {
          if (__builtin_amdgcn_readfirstlane(__hip_atomic_load((unsigned*)(smem + L_MISC), __ATOMIC_RELAXED, __HIP_MEMORY_SCOPE_WORKGROUP)) == 8u) break;
          __builtin_amdgcn_s_sleep(1);
        }
        asm volatile("" ::: "memory");
      }
      if constexpr (dat(k) >= 0) dma_e(IC<dat(k)>{});
      if constexpr (xat(k) >= 0) {
        constexpr int i = xat(k);
        X[i] = BLD128C(rs_ws, xl16 + (i & 3) * 1024, WS_X + (bid * 256 + w * 32) * 256 + (i >> 2) * 4096);
        SCHED();
      }
      if constexpr (rat(k) >= 0) {
        constexpr int f = rat(k);
        if constexpr ((f & 3) == 0) asm volatile("s_waitcnt vmcnt(%0)" :: "i"(younger(k, f < 24 ? f + 3 : 24)) : "memory");
        if constexpr (f >= 24) er[f & 3] = (xl16 & 48) ? L_MISC + 16 - 8192 : er[f & 3];
        read_e(IC<f>{});
      }
      if constexpr (k == KF - 8) { slo = make_sel((xl16 >> 4) & 15, 0); RESERVE4(z[0]); RESERVE4(z[1]); }
      if constexpr (k == KF - 6) shi = make_sel((xl16 >> 4) & 15, 1);
      if constexpr (k >= KF - 4 && k < KF + 44) {
        constexpr int j = (k - KF + 4) / 12, p = (k - KF + 4) % 12;
        if constexpr (p == 0) { SCHED(); mf<true>(z[j & 1], slo, X[2 * j]); }
        if constexpr (p == 1) { SCHED(); mf<false>(z[j & 1], shi, X[2 * j + 1]); }
      }
      if constexpr (k >= KF && k <= KF + 48) {
        constexpr int v = (k - KF) / 3, p = (k - KF) % 3, j = v >> 2, r = v & 3;
        if constexpr (p == 0 && v > 0) { s0 = __builtin_fmaf(ya, yb, s0); opaque(s0); }
        if constexpr (p == 0 && v < 16) { ya = gelu_arg(z[j & 1][r]); opaque(ya); }
        if constexpr (p == 1) { ya = __builtin_amdgcn_exp2f(ya); opaque(ya); yb = z[j & 1][r] * ((r & 1) ? bfhi(ww[j][r >> 1]) : bf2f(ww[j][r >> 1])); opaque(yb); }
        if constexpr (p == 2) { ya = __builtin_amdgcn_rcpf(1.f + ya); opaque(ya); }
      }
    };
    const float lp0 = pnn(E, IC<1>{}, side);
    __builtin_amdgcn_s_setprio(old ? POP2 : PYP2);
    STAMP(15);
    const float lp1 = pnn(E2, IC<0>{}, [&](auto K) { side(IC<K.v + 120>{}); });
    STAMP(20);
    const int lane = fresh_lane(), c = lane & 15;
#pragma unroll 1
    for (int spins = 0; spins < (1 << 14) && __any(s0 != s0); ++spins) {
      unsigned mx = 0;
#pragma unroll
      for (int k = 0; k < 8; ++k) mx = umax_(umax_(umax_(mx, X[k][0]), X[k][1]), umax_(X[k][2], X[k][3]));
      if (!__any(mx == ~0u)) break;
      asm volatile("" ::: "memory");
      load_x();
      s0 = 0.f;
#pragma unroll
      for (int j = 0; j < 4; ++j) {
        const f32x4 y = MFMA(shi, X[2 * j + 1], MFMA(slo, X[2 * j], ZERO4));
#pragma unroll
        for (int r = 0; r < 4; ++r)
          s0 = __builtin_fmaf(__builtin_amdgcn_rcpf(1.f + __builtin_amdgcn_exp2f(gelu_arg(y[r]))), y[r] * ((r & 1) ? bfhi(ww[j][r >> 1]) : bf2f(ww[j][r >> 1])), s0);
      }
    }
    STAMP(16);
    s0 += __uint_as_float(__builtin_amdgcn_mov_dpp(__float_as_uint(s0), 0x128, 0xf, 0xf, true));
    float* red = (float*)(smem + L_RED);
    if (c < 8) red[(((lane >> 4) & 1) * 8 + c) * 20 + 2 * w + (lane >> 5)] = s0;
    if (c == 0) { red[(2 * w) * 20 + 16 + (lane >> 4)] = lp0; red[(2 * w + 1) * 20 + 16 + (lane >> 4)] = lp1; }
    STAMP(17);
    BAR();
    if (old && w == 0 && lane < 16) {
      const f32x4* rp = (const f32x4*)(red + lane * 20);
      const f32x4 a = (rp[0] + rp[1]) + (rp[2] + rp[3]), b = rp[4];
      const float ld = rbf(rbf((a[0] + a[1]) + (a[2] + a[3])) + bf2f(b2[0]));
      out[bid * 16 + lane] = (u16)(pack2(rbf((b[0] + b[1]) + (b[2] + b[3])) + ld, 0.f) & 0xffffu);
      if (bid == 0 && lane == 0) __hip_atomic_store((gu64*)(ws + WS_EPOCH), tag, RLX_AGENT);
    }
    STAMP(18);
    RSTAMP(63);
  };
  if (w < 4) {
    gather3(IC<1>{});
    rest(IC<1>{});
    asm volatile("s_endpgm" ::: "memory");
#pragma unroll
    for (int i = 0; i < 16; ++i) RESERVE(acc[i]);
#pragma unroll
    for (int i = 0; i < 24; ++i) RESERVE(E[i >> 2][i & 3]);
#pragma unroll
    for (int i = 0; i < IXD; ++i) RESERVE(ix[i]);
    RESERVE(E[6][0]); RESERVE(T[0][0]); RESERVE(T[0][3]);
  }
  gather3(IC<0>{});
  rest(IC<0>{});
  asm volatile(".Lcode_end:");
}

extern "C" void kernel_launch(void* const* d_in, const int* in_sizes, int n_in, void* d_out, int out_size,
                              void* d_ws, size_t ws_size, hipStream_t stream) {
  k_fused<<<256, 512, 0, stream>>>((const int*)d_in[0], (const char*)d_in[1], (const u16*)d_in[2], (const char*)d_in[3],
                                   (const u16*)d_in[4], (const u16*)d_in[5], (const u16*)d_in[6], (char*)d_ws, (u16*)d_out);
}
```

```cpp
#include <hip/hip_runtime.h>
#include <hip/hip_bf16.h>
#include <stdint.h>

typedef short bf16x8 __attribute__((ext_vector_type(8)));
typedef float f32x4 __attribute__((ext_vector_type(4)));
typedef float f32x2 __attribute__((ext_vector_type(2)));
typedef __bf16 bf16x2 __attribute__((ext_vector_type(2)));
typedef unsigned u32x4 __attribute__((ext_vector_type(4)));
typedef unsigned u32x2 __attribute__((ext_vector_type(2)));
typedef unsigned u32x8 __attribute__((ext_vector_type(8)));
typedef short s16x4 __attribute__((ext_vector_type(4)));
typedef unsigned short u16;
typedef unsigned long long u64;
typedef u64 u64x8 __attribute__((ext_vector_type(8)));
typedef __attribute__((address_space(1))) u64 gu64;
typedef __amdgpu_buffer_rsrc_t rsrc_t;

#define NSTAMP 23
constexpr int NB = 4096, NF = 100, ND = 128, NV = 1000, NH = 512;

constexpr int WS_EPOCH = 0;
constexpr int WS_F1   = 16384;
constexpr int WS_LX   = 32768;
constexpr int WS_WT   = 65536;
constexpr int WS_LIST = 131072;
#define F2L 0
constexpr int WS_X    = (1 << 20) + F2L * 2048 * 128;
constexpr int WS_F2   = F2L ? 1 << 20 : WS_X + 2048 * 6144;
#define YST 8192
constexpr int WS_Y    = (1 << 20) + 2048 * (6144 + 128);
constexpr int WS_PN   = WS_Y + 256 * YST;
constexpr int WS_END  = WS_PN + 2048 * 1024;
constexpr size_t WS_STAMP = 32u << 20;

#define STAMP(k)
#define STAMPN(k)
#define STAMPNEND()
#define RSTAMP(k)
#define GOST
#define GOSO
#define RST(k)

#define S1(k)
#define S1END()
#define SF(i)
#define SFEND()
#define TSTAMP(k) STAMP(k)
#define GSTAMP(k)
#define CK_(k)
constexpr int L_P  = 0;
constexpr int L_PX = 131072;
constexpr int WBS  = 8224;
constexpr int L_WB = L_PX + 1008 * 16;
constexpr int L_WX = L_WB + WBS;
constexpr int L_MISC = L_WB + 2 * WBS;
constexpr int L_TOTAL = 163840;
constexpr int L_WT = L_PX;
constexpr int L_RED = L_WB + WBS;
static_assert(L_MISC + 32 <= L_TOTAL && L_WT + 23 * 1024 <= L_RED && L_MISC % 16 == 0 && L_RED % 16 == 0, "lds");

#define MFMA(a, b, c) __builtin_amdgcn_mfma_f32_16x16x32_bf16(__builtin_bit_cast(bf16x8, a), __builtin_bit_cast(bf16x8, b), c, 0, 0, 0)
template <bool FIRST> __device__ __forceinline__ void mf(f32x4& d, const u32x4& a, const u32x4& b) {
  if constexpr (FIRST) asm volatile("v_mfma_f32_16x16x32_bf16 %0, %1, %2, 0" : "+v"(d) : "v"(a), "v"(b));
  else asm volatile("v_mfma_f32_16x16x32_bf16 %0, %1, %2, %0" : "+v"(d) : "v"(a), "v"(b));
}
#define QUADS(X) X(8, "32:35") X(9, "36:39") X(10, "40:43") X(11, "44:47") X(12, "48:51") X(13, "52:55") X(14, "56:59") X(15, "60:63") X(16, "64:67") X(17, "68:71") \
  X(18, "72:75") X(19, "76:79") X(20, "80:83") X(21, "84:87") X(22, "88:91") X(23, "92:95") X(24, "96:99") X(25, "100:103") X(26, "104:107") X(27, "108:111") \
  X(28, "112:115") X(29, "116:119") X(30, "120:123") X(31, "124:127") X(32, "128:131") X(33, "132:135") X(34, "136:139") X(35, "140:143") X(36, "144:147") \
  X(37, "148:151") X(38, "152:155") X(39, "156:159") X(40, "160:163") X(41, "164:167") X(42, "168:171") X(43, "172:175") X(44, "176:179") X(45, "180:183") \
  X(46, "184:187") X(47, "188:191") X(48, "192:195") X(49, "196:199") X(50, "200:203") X(51, "204:207") X(52, "208:211") X(53, "212:215") X(54, "216:219") \
  X(55, "220:223") X(56, "224:227") X(57, "228:231") X(58, "232:235") X(59, "236:239") X(60, "240:243") X(61, "244:247") X(62, "248:251") X(63, "252:255")
template <int R, class V> __device__ __forceinline__ void pinq(const V& x) {
#define X(i, r) if constexpr (R == i) asm volatile("" :: "{v[" r "]}"(x));
  QUADS(X)
#undef X
}
#define NTK 0
#define NTS "nt"
#define NT 0
template <int OFF, bool N = false> __device__ __forceinline__ void dma16(unsigned vo, int lds, rsrc_t rs, int so) {
  if constexpr (N) asm volatile("s_mov_b32 m0, %1\n\ts_nop 0\n\tbuffer_load_dwordx4 %0, %2, %3 offen offset:%4 " NTS " lds" :: "v"(vo), "s"(lds), "s"(rs), "s"(so), "i"(OFF) : "memory");
  else asm volatile("s_mov_b32 m0, %1\n\ts_nop 0\n\tbuffer_load_dwordx4 %0, %2, %3 offen offset:%4 lds" :: "v"(vo), "s"(lds), "s"(rs), "s"(so), "i"(OFF) : "memory");
}
__device__ __forceinline__ void set_m0(int lds) { asm volatile("s_mov_b32 m0, %0\n\ts_nop 0" :: "s"(lds) : "memory"); }
template <int OFF> __device__ __forceinline__ void dma16n(unsigned vo, rsrc_t rs, int so) {
   asm volatile("buffer_load_dwordx4 %0, %1, %2 offen offset:%3 lds" :: "v"(vo), "s"(rs), "s"(so), "i"(OFF) : "memory");
}
template <class X> __device__ __forceinline__ void use(const X& x) { asm volatile("" :: "v"(x) : "memory"); }
template <int OFF> __device__ __forceinline__ void ld16(u32x4& x, unsigned vo, rsrc_t rs, int so) {
  asm volatile("buffer_load_dwordx4 %0, %1, %2, %3 offen offset:%4" : "=v"(x) : "v"(vo), "s"(rs), "s"(so), "i"(OFF));
}
template <int N> __device__ __forceinline__ void wait4(u32x4 (&x)[4]) {
  asm volatile("s_waitcnt vmcnt(%4)" : "+v"(x[0]), "+v"(x[1]), "+v"(x[2]), "+v"(x[3]) : "i"(N));
}
template <class X> __device__ __forceinline__ void hold(const X& x) { asm volatile("" :: "v"(x)); }
__device__ __forceinline__ unsigned pick(unsigned a, unsigned b, unsigned long long m) {
  const unsigned r = __builtin_amdgcn_inverse_ballot_w64(m) ? b : a; hold(r); return r;
}
template <class X> __device__ __forceinline__ void opaque(X& x) { asm volatile("" : "+v"(x)); }
#define RESERVE(x) asm volatile("" : "=v"(x))
#define ACCS(X) X(0, "192:195") X(1, "196:199") X(2, "200:203") X(3, "204:207") X(4, "208:211") X(5, "212:215") X(6, "216:219") X(7, "220:223") \
  X(8, "224:227") X(9, "228:231") X(10, "232:235") X(11, "236:239") X(12, "240:243") X(13, "244:247") X(14, "248:251") X(15, "252:255")
template <int I> __device__ __forceinline__ void acc_init(f32x4& d, const u32x4& a, const u32x4& b, const f32x4& c) {
#define X(i, r) if constexpr (I == i) asm volatile("v_mfma_f32_16x16x32_bf16 %0, %1, %2, %3" : "=&{v[" r "]}"(d) : "v"(a), "v"(b), "v"(c));
  ACCS(X)
#undef X
}
template <int I> __device__ __forceinline__ void acc_more(f32x4& d, const u32x4& a, const u32x4& b) {
#define X(i, r) if constexpr (I == i) asm volatile("v_mfma_f32_16x16x32_bf16 %0, %1, %2, %0" : "+{v[" r "]}"(d) : "v"(a), "v"(b));
  ACCS(X)
#undef X
}
template <int I> __device__ __forceinline__ void acc_pin(f32x4& d) {
#define X(i, r) if constexpr (I == i) asm volatile("" : "+{v[" r "]}"(d));
  ACCS(X)
#undef X
}
template <int I> __device__ __forceinline__ void acc_add(f32x4& d, const u32x4& a, const u32x4& b0, const u32x4& b1, unsigned si) {
  const u32x8 b = __builtin_shufflevector(b0, b1, 0, 1, 2, 3, 4, 5, 6, 7);
#define X(i, r) if constexpr (I == i) asm volatile("v_smfmac_f32_16x16x64_bf16 %0, %1, %2, %3" : "+{v[" r "]}"(d) : "v"(a), "v"(b), "v"(si));
  ACCS(X)
#undef X
}
template <int OFF> __device__ __forceinline__ void wat(unsigned x) { asm volatile("ds_write_addtid_b32 %0 offset:%1" :: "v"(x), "i"(OFF) : "memory"); }
__device__ __forceinline__ u32x4 rdtr(const char* p, int step) {
  typedef __attribute__((address_space(3))) s16x4* lp;
  const u32x2 a = __builtin_bit_cast(u32x2, __builtin_amdgcn_ds_read_tr16_b64_v4i16((lp)p)), b = __builtin_bit_cast(u32x2, __builtin_amdgcn_ds_read_tr16_b64_v4i16((lp)(p + step)));
  return (u32x4){a[0], a[1], b[0], b[1]};
}
__device__ __forceinline__ void dma16c(unsigned vo, int lds, rsrc_t rs, int so) {
  asm volatile("s_mov_b32 m0, %1\n\ts_nop 0\n\tbuffer_load_dwordx4 %0, %2, %3 offen sc1 lds" :: "v"(vo), "s"(lds), "s"(rs), "s"(so) : "memory");
}
template <unsigned LANES, bool C> __device__ __forceinline__ void touch(unsigned vo, rsrc_t rs, int so) {
  if constexpr (C) asm volatile("s_mov_b32 m0, %3\n\ts_mov_b64 exec, %4\n\tbuffer_load_dword %0, %1, %2 offen sc1 lds\n\ts_mov_b64 exec, -1" :: "v"(vo), "s"(rs), "s"(so), "i"(L_MISC + 64), "s"((u64)LANES) : "memory");
  else if constexpr (NT & 16) asm volatile("s_mov_b32 m0, %3\n\ts_mov_b64 exec, %4\n\tbuffer_load_dword %0, %1, %2 offen " NTS " lds\n\ts_mov_b64 exec, -1" :: "v"(vo), "s"(rs), "s"(so), "i"(L_MISC + 64), "s"((u64)LANES) : "memory");
  else asm volatile("s_mov_b32 m0, %3\n\ts_mov_b64 exec, %4\n\tbuffer_load_dword %0, %1, %2 offen lds\n\ts_mov_b64 exec, -1" :: "v"(vo), "s"(rs), "s"(so), "i"(L_MISC + 64), "s"((u64)LANES) : "memory");
}
template <int OFF> __device__ __forceinline__ void ld16c(u32x4& x, unsigned vo, rsrc_t rs, int so) {
  asm volatile("buffer_load_dwordx4 %0, %1, %2, %3 offen offset:%4 sc1" : "=v"(x) : "v"(vo), "s"(rs), "s"(so), "i"(OFF) : "memory");
}
template <int OFF> __device__ __forceinline__ void ld8(u32x2& x, unsigned vo, rsrc_t rs, int so) {
  asm volatile("buffer_load_dwordx2 %0, %1, %2, %3 offen offset:%4" : "=v"(x) : "v"(vo), "s"(rs), "s"(so), "i"(OFF));
}
__device__ __forceinline__ unsigned allones() { unsigned r; asm volatile("v_mov_b32 %0, -1" : "=v"(r)); return r; }
template <int N, int M> __device__ __forceinline__ void vmwait2(int w) {
  if constexpr (M == 0) asm volatile("s_waitcnt vmcnt(%0)" :: "i"(N) : "memory");
  else asm volatile("s_waitcnt vmcnt(%1)\n\ts_cmp_lt_u32 %2, 4\n\ts_cbranch_scc1 .Lv%=\n\ts_waitcnt vmcnt(%0)\n.Lv%=:" :: "i"(N), "i"(N + M), "s"(w) : "memory", "scc");
}
template <int N> __device__ __forceinline__ void vmwait() { asm volatile("s_waitcnt vmcnt(%0)" :: "i"(N) : "memory"); }
template <int M> __device__ __forceinline__ void pin4(u32x4 (&x)[4]) {
  if constexpr (M == 0) asm volatile("" : "+{v[64:67]}"(x[0]), "+{v[68:71]}"(x[1]), "+{v[72:75]}"(x[2]), "+{v[76:79]}"(x[3]));
  if constexpr (M == 1) asm volatile("" : "+{v[80:83]}"(x[0]), "+{v[84:87]}"(x[1]), "+{v[88:91]}"(x[2]), "+{v[92:95]}"(x[3]));
  if constexpr (M == 2) asm volatile("" : "+{v[96:99]}"(x[0]), "+{v[100:103]}"(x[1]), "+{v[104:107]}"(x[2]), "+{v[108:111]}"(x[3]));
  if constexpr (M == 3) asm volatile("" : "+{v[112:115]}"(x[0]), "+{v[116:119]}"(x[1]), "+{v[120:123]}"(x[2]), "+{v[124:127]}"(x[3]));
  if constexpr (M == 4) asm volatile("" : "+{v[128:131]}"(x[0]), "+{v[132:135]}"(x[1]), "+{v[136:139]}"(x[2]), "+{v[140:143]}"(x[3]));
  if constexpr (M == 5) asm volatile("" : "+{v[144:147]}"(x[0]), "+{v[148:151]}"(x[1]), "+{v[152:155]}"(x[2]), "+{v[156:159]}"(x[3]));
  if constexpr (M == 6) asm volatile("" : "+{v[160:163]}"(x[0]), "+{v[164:167]}"(x[1]), "+{v[168:171]}"(x[2]), "+{v[172:175]}"(x[3]));
  if constexpr (M == 7) asm volatile("" : "+{v[176:179]}"(x[0]), "+{v[180:183]}"(x[1]), "+{v[184:187]}"(x[2]), "+{v[188:191]}"(x[3]));
  if constexpr (M == 8) asm volatile("" : "+{v[192:195]}"(x[0]), "+{v[196:199]}"(x[1]), "+{v[200:203]}"(x[2]), "+{v[204:207]}"(x[3]));
  if constexpr (M == 9) asm volatile("" : "+{v[208:211]}"(x[0]), "+{v[212:215]}"(x[1]), "+{v[216:219]}"(x[2]), "+{v[220:223]}"(x[3]));
  if constexpr (M == 10) asm volatile("" : "+{v[224:227]}"(x[0]), "+{v[228:231]}"(x[1]), "+{v[232:235]}"(x[2]), "+{v[236:239]}"(x[3]));
  if constexpr (M == 11) asm volatile("" : "+{v[240:243]}"(x[0]), "+{v[244:247]}"(x[1]), "+{v[248:251]}"(x[2]), "+{v[252:255]}"(x[3]));
}
#define BAR() asm volatile("s_waitcnt lgkmcnt(0)\n\ts_barrier" ::: "memory")
#define SCHED() __builtin_amdgcn_sched_barrier(0)
#define DRAIN() asm volatile("s_waitcnt vmcnt(0)" ::: "memory")
#define RLX_AGENT __ATOMIC_RELAXED, __HIP_MEMORY_SCOPE_AGENT
constexpr int SC1 = 16;
#define BLD128(rs, vo, so) __builtin_bit_cast(u32x4, __builtin_amdgcn_raw_buffer_load_b128(rs, vo, so, 0))
#define BLD128C(rs, vo, so) __builtin_bit_cast(u32x4, __builtin_amdgcn_raw_buffer_load_b128(rs, vo, so, SC1))
#define BST128C(v, rs, vo, so) do { const u32x4 v_ = v; __builtin_amdgcn_raw_buffer_store_b128(v_, rs, vo, so, SC1); asm volatile("s_nop 1" :: "v"(v_)); } while (0)
#define ZERO4 ((f32x4){0.f, 0.f, 0.f, 0.f})

template <int I> struct IC { static constexpr int v = I; };
template <int B, int E, class F> __device__ __forceinline__ void sfor(F&& f) {
  if constexpr (B < E) { f(IC<B>{}); sfor<B + 1, E>(f); }
}
__device__ __forceinline__ rsrc_t mkrs(const void* p, unsigned bytes) {
  return __builtin_amdgcn_make_buffer_rsrc((void*)p, 0, bytes, 0x00020000);
}
__device__ __forceinline__ unsigned pack2(float a, float b) {
  f32x2 v = {a, b};
  return __builtin_bit_cast(unsigned, __builtin_convertvector(v, bf16x2));
}
__device__ __forceinline__ unsigned umin_(unsigned a, unsigned b) { return a < b ? a : b; }
__device__ __forceinline__ unsigned umax_(unsigned a, unsigned b) { return a > b ? a : b; }
__device__ __forceinline__ float bf2f(unsigned x16) { return __uint_as_float(x16 << 16); }
__device__ __forceinline__ float bfhi(unsigned x) { return __uint_as_float(x & 0xffff0000u); }
__device__ __forceinline__ float rbf(float x) { return __uint_as_float(pack2(x, 0.f) << 16); }
__device__ __forceinline__ int fresh_lane() {
  unsigned z;
  asm volatile("s_mov_b32 %0, 0" : "=s"(z));
  const int l = (int)__builtin_amdgcn_mbcnt_hi(~0u, __builtin_amdgcn_mbcnt_lo(~0u, z));
  __builtin_assume(l >= 0 && l < 64);
  return l;
}
__device__ __forceinline__ u32x4 make_sel(int c, int q) {
  u32x4 sel;
  const int e = c - 8 * (q & 1);
  const unsigned one = (e & 1) ? 0x3F800000u : 0x3F80u;
#pragma unroll
  for (int k = 0; k < 4; ++k) sel[k] = ((e >> 1) == k) ? one : 0u;
  asm volatile("s_nop 1" : "+v"(sel));
  return sel;
}
__device__ __forceinline__ unsigned half16(unsigned x, int hi) {
  unsigned r;
  if (hi) asm("v_lshlrev_b32_sdwa %0, 4, %1 dst_sel:DWORD dst_unused:UNUSED_PAD src0_sel:DWORD src1_sel:WORD_1" : "=v"(r) : "v"(x));
  else asm("v_lshlrev_b32_sdwa %0, 4, %1 dst_sel:DWORD dst_unused:UNUSED_PAD src0_sel:DWORD src1_sel:WORD_0" : "=v"(r) : "v"(x));
  return r;
}
struct QT { u32x4 t0, t2; };
__device__ __forceinline__ void qt_init(QT& t) { asm volatile("s_nop 7" : "=v"(t.t0), "=v"(t.t2)); }
#define CNDX(perm, o, own, oth, m) do { register u64 m_ asm("vcc") = m; unsigned o_ = o; \
  asm volatile("v_cndmask_b32_dpp %0, %2, %1, vcc quad_perm:" perm " row_mask:0xf bank_mask:0xf" : "+v"(o_) : "v"(own), "v"(oth), "s"(m_)); o = o_; } while (0)
#define CNDXI(perm, own, oth, m) do { register u64 m_ asm("vcc") = m; unsigned o_ = own; \
  asm volatile("v_cndmask_b32_dpp %0, %1, %0, vcc quad_perm:" perm " row_mask:0xf bank_mask:0xf" : "+v"(o_) : "v"(oth), "s"(m_)); own = o_; } while (0)
__device__ __forceinline__ void qt_step(u32x4 (&x)[4], QT& t, int n) {
  const int st = n >> 3, p = (n >> 2) & 1, d = n & 3;
  if (st == 0) {
    if (p == 0) CNDX("[1,0,3,2]", t.t0[d], x[0][d], x[1][d], 0x5555555555555555ull);
    else        CNDX("[1,0,3,2]", t.t2[d], x[2][d], x[3][d], 0x5555555555555555ull);
  } else if (st == 1) {
    if (p == 0) CNDXI("[1,0,3,2]", x[3][d], x[2][d], 0xAAAAAAAAAAAAAAAAull);
    else        CNDXI("[1,0,3,2]", x[1][d], x[0][d], 0xAAAAAAAAAAAAAAAAull);
  } else if (st == 2) {
    if (p == 0) CNDX("[2,3,0,1]", x[0][d], t.t0[d], t.t2[d], 0x3333333333333333ull);
    else        CNDX("[2,3,0,1]", x[2][d], x[1][d], x[3][d], 0x3333333333333333ull);
  } else {
    if (p == 0) CNDXI("[2,3,0,1]", t.t2[d], t.t0[d], 0xCCCCCCCCCCCCCCCCull);
    else        CNDXI("[2,3,0,1]", x[3][d], x[1][d], 0xCCCCCCCCCCCCCCCCull);
  }
  if (n == 31) { const u32x4 dead = x[1]; x[1] = x[2]; x[2] = t.t2; t.t2 = dead; asm volatile("s_nop 1"); SCHED(); }
}
__device__ __forceinline__ void qt_all(u32x4 (&x)[4], QT& t) {
#pragma unroll
  for (int n = 0; n < 32; ++n) qt_step(x, t, n);
}
__device__ __forceinline__ void wait_flags(const char* p, u64 tag, int step = 8, int mask = 31) {
  gu64* f = (gu64*)(p + (fresh_lane() & mask) * step);
#pragma unroll 1
  for (int spins = 0; spins < (1 << 16); ++spins) {
    const u64 v = __hip_atomic_load(f, RLX_AGENT);
    if (__all(v == tag)) break;
    __builtin_amdgcn_s_sleep(2);
  }
  __builtin_amdgcn_fence(__ATOMIC_ACQUIRE, "wavefront");
}
__device__ __forceinline__ float gelu_arg(float x) {
  return x * __builtin_fmaf(x * x, -2.f * 0.7978845608028654f * 0.044715f * 1.4426950408889634f, -2.f * 0.7978845608028654f * 1.4426950408889634f);
}
__device__ __forceinline__ void RESERVE4(f32x4& x) { asm volatile("" : "=v"(x)); }

__device__ __forceinline__ float row_sum(float x) {
  x += __uint_as_float(__builtin_amdgcn_mov_dpp(__float_as_uint(x), 0xB1, 0xf, 0xf, true));
  x += __uint_as_float(__builtin_amdgcn_mov_dpp(__float_as_uint(x), 0x4E, 0xf, 0xf, true));
  x += __uint_as_float(__builtin_amdgcn_mov_dpp(__float_as_uint(x), 0x141, 0xf, 0xf, true));
  x += __uint_as_float(__builtin_amdgcn_mov_dpp(__float_as_uint(x), 0x140, 0xf, 0xf, true));
  return x;
}

#define POG 0
#define PYG 1
#define ROT 1
#define RD 5
#define RD5 4
#define WN0 12
#define SDN 10
#define GPO 1
#define PRC 0
#define PRG 1
#define PRCB 1
#define PRGB PRG
#define PRW 2

#define B0 0
#define NPRE 8
#define NQ 12
#define TP 11
#define NPA 0
#define SD 1
#define RM 0x3c
#define PY0 0
#define KW 0
#define KR 2
#define SR 3
#define TFL 50
#define TW 82
#define TCK 66
#define TT -1
#define TN -1
#define UIX 1
#define T7 -1
#define XRA 4
#define XRB 4
#define XP RD
#define PXA 2
#define PXB 1
#define ST0 0
#define SH 0
#define XTW 5
#define NB4 0
#define WB4 4
#define AB 0
#define GX 0
#define FR 1
#define FRB "s_barrier\n\t"
#define TTL 8
#define PTW 6
#define PTL 1
#define SDT SDN
#define SDU 2
#define FT 0
#define T2W -1
#define T2P 0
#define TTP 1
#define WTJ -1
#define SW 6
#define SWS 0
#define SWW 7
#define SWX "s_load_dword %0, %7, 0\n\ts_load_dword %0, %9, 0\n\ts_load_dword %0, %10, 0\n"
#define LA 0
#define TPA 10
#define TTW 4
#define CTW 5
#define CTL 13
constexpr bool viareg(int b) { return (RM >> b) & 1; }
constexpr int tslot(int b) { int n = 0; for (int i = 0; i < b; ++i) n += !viareg(i); return n * 4096; }
constexpr int rqt(int o) { return o < NQ ? -2 : KR + (o - NQ) * SR / SD; }
constexpr int rqat(int t) { for (int o = NQ; o < 32; ++o) if (rqt(o) == t) return o; return -1; }
constexpr int since(int t0, int t) { int n = 0; for (int o = NQ; o < 32; ++o) n += rqt(o) > t0 && rqt(o) < t; return n; }
constexpr int others(int t0, int t) { return 2 * (TFL >= t0 && TFL < t) + 6 * (TCK >= t0 && TCK < t) + 2 * (TW >= t0 && TW < t) + (T7 < 0 ? 3 : 2) * (TT >= t0 && TT < t && TT >= 0); }
constexpr int behind(int b, int t) {
  int n = 0;
  for (int o = 4 * b + 4; o < 32; ++o) n += rqt(o) < t;
  return n + (TFL >= rqt(4 * b + 3) && TFL < t);
}
constexpr bool early() { for (int b = 1; b < 8; ++b) if (rqt(4 * b + 3) >= 12 * (b - 1)) return false; return SR >= SD && NPRE <= NQ && NPA <= NPRE; }
constexpr bool places_ok() { for (int b = 0; b < 8; ++b) if (!viareg(b) && (tslot(b) > 12288 || (tslot(b) < 8192 && b > tslot(b) / 1024 + 1))) return false; return !viareg(0); }
static_assert(!viareg(1) && places_ok() && KW + 3 < 12 && early() && TCK > TFL && TCK < 84 && (TT < 0 || TT > TCK) && TT != TW && TW != TCK && TW != TFL, "order");
static_assert(AB == 0 || (!LA && NPA == 0 && !viareg(AB - 1) && 4 * AB <= NQ), "ab");
constexpr int TC = TW < TCK ? TW : TCK;

#define KP -3
#define KPB -2
#define TWB TW0
#define KM 180
#define XTS 2, 4, 6, 8, 10, 12
#define XBS XTS
#define AC0 1
#define ACS 2
#define KFA 14
#define KXA 20
#define KGA 80
#define KFB 30
#define KXB 50
#define KGB 126
#define CSA 6
#define NCA 5
#define CSB 10
#define NCB 5
#define YSP (1 << 16)
#define ZR 1
#define YSL 4
#define KY 165
#define KYB 196
#define KMB 190
#define KE 223
#define KN 240
#define GS 0
#define PLA -1, 0
#define PLB -1, 1, 160, 0
#define SLP 2
#define RDB RD5
#define ERS -24, -22, -20, -17, -14, -11, -8, -4, 0, 4, 8, 11, 14, 17
#define TW0 1
#define TWS 2
#define WL 2
#define LAG 4
#define NWI 0
#define NW0 NWI
#define NEI 0
#define PRE 1
#define UW 4
#define NAR 2
#define WNB 12
#define NCL 2
#define IB0 4
#define IB1 7
#define NI0 2
#define RQU -1
#define ERD 0
#define EK0 3
#define NBW 1
#define YM 1
#define PT 0
#define PJ 2
#define PC true
#define PM 0x1ffff
#define PG 0
#define ET 0
#define EC true
#define IS 8
#define YC 0
#define YCJ 2
#define YCN 11
#define NE0 3
constexpr int TI[28] = {6, 0, 0, 1, 0, 1, 2, 0, 1, 3, 0, 1, 4, 0, 1, 5, 0, 1, 2, 2, 2, 2, 3, 3, 3, 4, 4, 5};
constexpr int TJ[28] = {6, 6, 0, 6, 1, 1, 6, 2, 2, 6, 3, 3, 6, 4, 4, 6, 5, 5, 2, 3, 4, 5, 3, 4, 5, 4, 5, 5};
constexpr int eb(int f) { return (f / 2 + 6) % 7; }
constexpr int wti(int u) { return TJ[u] < 6 ? TJ[u] * (TJ[u] + 1) / 2 + TI[u] : 21 + (TI[u] >> 2); }
constexpr bool wnew(int u) { if (u > 27) return false; for (int i = 0; i < u; ++i) if (wti(i) == wti(u)) return false; return true; }
constexpr int wcount(int a, int b) { int n = 0; for (int u = a; u < b; ++u) n += wnew(u); return n; }
constexpr int ER[14] = {ERS}, XT[6] = {XTS}, XB[6] = {XBS};
constexpr int KS = 2 * (57 + LAG) + 1;
constexpr int erat(int k) { for (int s = 0; s < 4; ++s) for (int f = 0; f < 14; ++f) if (56 * s + ER[f] == k) return 16 * s + f; return -1; }
constexpr int level(bool b, int k) {
  constexpr int pa[] = {PLA}, pb[] = {PLB};
  for (int i = 0; i < (int)((b ? sizeof(pb) : sizeof(pa)) / sizeof(int)); i += 2) if ((b ? pb : pa)[i] == k) return (b ? pb : pa)[i + 1];
  return -1;
}
constexpr bool pnn_ok() {
  for (int f = 0; f < 14; ++f) {
    if (f && ER[f] <= ER[f - 1]) return false;
    for (int u = 0; u < 28; ++u) if ((TI[u] == eb(f) || TJ[u] == eb(f)) && (ER[f] >= 2 * u + (f & 1) || ER[f] + 56 < 2 * u + (f & 1))) return false;
  }
  for (int i = 0; i < 6; ++i) if (XT[i] < 2 * i + 2 || XB[i] < 2 * i + 2 || XT[i] > KP + 20 || XB[i] > KPB + 20) return false;
  return ER[13] < ER[0] + 56 && KP <= KFA && KFA < KXA && KXA + (NCA - 1) * CSA < KGA - 30 && KGA + 3 * GS < KY && KPB <= KFB && KFB < KXB && KXB + (NCB - 1) * CSB < KGB - 30 && KGB + 3 * GS < KYB;
}
static_assert(pnn_ok() && IB1 >= RQU && IB1 >= IB0, "final");

typedef unsigned sj_t;
template <int I, int N> __device__ __forceinline__ void stouch(sj_t& sj, u64 b) {
  if constexpr (I < N) {
    if constexpr (SH) asm volatile("s_load_dwordx2 %0, %1, %2" : "+s"(sj) : "s"(b), "i"(I * 1024 + 60));
    else asm volatile("s_load_dword %0, %1, %2" : "+s"(sj) : "s"(b), "i"(I * 1024));
    stouch<I + 1, N>(sj, b);
  }
}
#define NTI ""
#define NTX ""
#define NTT ""
__global__ void __launch_bounds__(512, 2)
k_fused(const int*, const char*, const u16*, const char*, const u16*, const u16*, const u16*, char*, u16*) {
  __shared__ __attribute__((aligned(16))) char smem[L_TOTAL];
  u64x8 arg; u64 arg8;
  asm volatile("s_load_dwordx16 %0, %2, 0\n\ts_load_dwordx2 %1, %2, 64" : "=&s"(arg), "=&s"(arg8) : "s"(__builtin_amdgcn_kernarg_segment_ptr()));
  unsigned tc;
  {
    u64 pc0; unsigned csz;
    asm volatile("s_getpc_b64 %0\n.Lcode_begin:\n\ts_mov_b32 %1, .Lcode_end-.Lcode_begin" : "=s"(pc0), "=s"(csz));
    asm volatile("v_cmp_gt_u32 vcc, %4, %1\n\ts_and_b64 exec, exec, vcc\n\ts_cbranch_execz .Lnc%=\n\tbuffer_load_dword %0, %1, %2, %3 offen offset:256\n.Lnc%=:\n\ts_mov_b64 exec, -1"
                 : "=&v"(tc) : "v"(threadIdx.x * 128), "s"(mkrs((const void*)pc0, csz)), "s"((blockIdx.x >> 3) * (CTL * 128)), "s"(CTL * 128) : "vcc", "scc");
    SCHED();
  }
  int w = __builtin_amdgcn_readfirstlane(threadIdx.x >> 6);
  int bid = blockIdx.x, xl = bid & 7, rr = bid >> 3;
  int g = xl * 8 + (rr & 7), phi = rr >> 3, par = rr & 1;
  int wsb = phi * 24 * 128 * 1024 + (g ^ GX) * 16;
  int lsb = WS_LIST + phi * 6 * 32768 + w * 4096;
  int tsb = w * 128 * 256, slice = L_P + w * 16384;
  int wv = ROT ? (w + rr) & 7 : w;
  const int lk = (rr >> 2) * 2 + (xl >> 2);
  unsigned dv[4];
  int toff, l16, pa, pb, ps, wo, wm, wso, x0;
  {
    const int lane = threadIdx.x & 63;
    x0 = ((lane >> 2) & 2) + ((lane >> 3 ^ xl) & 1);
    const int po = (((lane >> 2) & 12) + ((lane >> 1) & 3)) * (NF * 4) + (lane & 1) * 16;
    pa = po + x0 * (16 * NF * 4); pb = po + (x0 ^ 5) * (16 * NF * 4);
    ps = (lk + (xl & 2) * 8) * 128 * NF * 4 + (rr & 3) * 96 + w * 32;
    wo = ((lane << 17) | (lane << 8)) & 0x63c00; wm = L_WB + (wv >> 1) * 2056 + ((wv ^ par) & 1) * 1024; wso = wsb + wv * 16384;
    l16 = lane * 128;
    if constexpr (FR) {
      if (w == 3) { pa = ((lane & 4) * 2 + (lane & 3)) * (NF * 4) + ((lane >> 3) & 3) * 4; ps = (128 * par + 8 * (bid >> 4) + xl) * 16 * NF * 4; }
    }
    asm volatile("" : "+v"(pa), "+v"(pb), "+v"(wo), "+v"(l16), "+v"(x0));
    if constexpr (NB4 > 0) {
      const int h = (lane & 15) ^ ((lane >> 4) << 2);
      dv[0] = (lane >> 4) * 256 + (h ^ ((h & 4) << 1)) * 16;
      dv[1] = dv[0] ^ 16; dv[2] = dv[0] ^ 32; dv[3] = dv[0] ^ 48;
      asm volatile("" : "+v"(dv[0]), "+v"(dv[1]), "+v"(dv[2]), "+v"(dv[3]), "+s"(tsb), "+s"(slice));
    }
    asm volatile("s_waitcnt lgkmcnt(0)" : "+s"(arg), "+s"(arg8), "+s"(ps), "+s"(wm), "+s"(wso));
  }
  unsigned sw = 0;
  if constexpr (SW == 4)
    asm volatile("s_cmp_lg_u32 %1, %8\n\ts_cbranch_scc1 .Lsw%=\n\ts_bitcmp1_b32 %7, 1\n\ts_cbranch_scc1 .L2%=\n\ts_bitcmp1_b32 %7, 0\n\ts_cbranch_scc1 .L1%=\n\ts_load_dword %0, %2, %5\n"
                 ".L1%=:\n\ts_load_dword %0, %2, %6\n\ts_branch .Lsw%=\n.L2%=:\n\ts_bitcmp1_b32 %7, 0\n\ts_cbranch_scc1 .L3%=\n\ts_load_dword %0, %3, 0\n.L3%=:\n\ts_load_dword %0, %4, 0\n.Lsw%=:"
                 : "=&{s95}"(sw) : "s"(w), "s"(arg[3]), "s"(arg[1]), "s"(arg[2]), "s"(wso), "s"(96 * ND * NH * 2), "s"(rr >> SWS), "i"(SWW) : "scc");
  else if constexpr (SW > 0)
    asm volatile("s_cmp_lg_u32 %1, %8\n\ts_cbranch_scc1 .Lsw%=\n\ts_load_dword %0, %2, %5\n\ts_load_dword %0, %2, %6\n\ts_load_dword %0, %3, 0\n\ts_load_dword %0, %4, 0\n\t" SWX ".Lsw%=:"
                 : "=&{s95}"(sw) : "s"(w), "s"(arg[3]), "s"(arg[1]), "s"(arg[2]), "s"(wso), "s"(96 * ND * NH * 2), "s"(arg[4]), "i"(SWW), "s"(arg[5]), "s"(arg8), "s"(arg[6]) : "scc");
  typedef __attribute__((address_space(1))) char* gp;
  char* const ws = (char*)(gp)arg[7];
  const void* const wp = (char*)(gp)arg[2], * const b1 = (char*)(gp)arg[4], * const w2 = (char*)(gp)arg[5];
  const rsrc_t rs_w1 = mkrs((char*)(gp)arg[3], NF * ND * NH * 2), rs_tab = mkrs((char*)(gp)arg[1], NV * ND * 2), rs_ws = mkrs(ws, WS_END);
  const rsrc_t rs_idx = mkrs((char*)(gp)arg[0], NB * NF * 4);
  u32x4 T[8][4];
  auto req_t = [&](auto O) {
    constexpr int m = O.v >> 2, i = O.v & 3;
    if constexpr (viareg(m)) ld16<i * 1024>(T[m][i], toff, rs_tab, tsb + m * 4096);
    else dma16<i * 1024>(dv[i], slice + tslot(m), rs_tab, tsb + m * 4096);
  };
  auto dma_w = [&](int j, int l16, int wv) {
    if ((NT & 8) || ((NT & 128) && j == 1)) dma16<0, true>(((l16 & 48) << 13) + ((l16 & ~63) << 4), L_WB + (j & 1) * WBS + (wv >> 1) * 2056 + ((wv ^ par) & 1) * 1024, rs_w1, wsb + j * 4 * 128 * 1024 + wv * 16384);
    else dma16<0>(((l16 & 48) << 13) + ((l16 & ~63) << 4), L_WB + (j & 1) * WBS + (wv >> 1) * 2056 + ((wv ^ par) & 1) * 1024, rs_w1, wsb + j * 4 * 128 * 1024 + wv * 16384);
  };
  u32x4 ix[4], lx;
  auto renew = [&]() { asm volatile("" : "+{v63}"(l16)); __builtin_assume(l16 >= 0 && l16 < 1024); };
  auto load_ix = [&](int j, int k0, int k1) {
    renew();
#pragma unroll
    for (int k = k0; k < k1; ++k) ix[k] = BLD128C(rs_ws, l16 + k * 1024, lsb + j * 32768);
  };
  u32x4 Wf[2][4];
  int wad;
  auto read_w = [&](int j, int i) { Wf[i & 1][i >> 1] = rdtr(smem + wad + (j & 1) * WBS + (i >> 1) * 512 + (i & 1) * 8, 256); };
  auto set_wad = [&]() { renew(); wad = L_WB + l16 + (l16 >> 8) * 1800; opaque(wad); };
  auto touches = [&]() {
    const int lane = fresh_lane();
    touch<0xffffff, true>((lane >> 2) * 32768 + (lane & 3) * 128, rs_ws, lsb + 16384 + (rr & 7) * 512);
    if constexpr (T7 < 0) touch<0xffff, false>(lane * 1024, rs_w1, wsb + (1024 + (rr & 7) * 64 + (w & 3) * 16) * 1024);
    touch<0xfffff, true>((lane >> 2) * 32768 + (lane & 3) * 128, rs_ws, lsb + 32768 + (rr & 7) * 512);
  };
  u32x2 bz;
  u64 tag;
  {
    u32x4 pi[2];
    unsigned tt, pj[2], k0, half, c2, base, xk, xh;
    int lso;
    if constexpr (XTW >= 0) { if (w == XTW) touch<0xffff, false>(l16 << 3, rs_w1, ((96 + phi) * 128 + (rr & 7) * 16) * 1024 + (g ^ GX) * 16); }
    auto req_i = [&]() {
      asm volatile("s_cmp_gt_u32 %[w], 2\n\ts_cbranch_scc1 .Lnl%=\n\tbuffer_load_dwordx4 %0, %2, %4, %5 offen\n\tbuffer_load_dwordx4 %1, %3, %4, %5 offen\n.Lnl%=:"
                   : "=&v"(pi[0]), "=&v"(pi[1]) : "v"(pa), "v"(pb), "s"(rs_idx), "s"(ps), [w] "s"(w) : "scc");
    };
    if constexpr (LA != 2) req_i();
    if constexpr (TTL == 64)
      asm volatile("s_cmp_lg_u32 %[w], %[tw]\n\ts_cbranch_scc1 .Lnt%=\n\tbuffer_load_dword %0, %1, %2, %3 offen\n.Lnt%=:"
                   : "=&v"(tt) : "v"(l16), "s"(rs_tab), "s"(rr * 8192), [w] "s"(w), [tw] "i"(TTW) : "scc");
    else {
      tt = 0;
      if constexpr (TTP) { if (w == TTW) touch<0xff, false>((l16 & 384) + ((l16 & 512) << 3), rs_tab, rr * 8192 + xl * 512); } else
      if (w == TTW) touch<(TTL == 32 ? 0xffffffffu : (1u << (TTL & 31)) - 1), false>(l16, rs_tab, (bid & (2048 / TTL - 1)) * (TTL * 128));
    }
    if constexpr (PTW >= 0) { if (w == PTW) touch<(PTL == 1 ? 1 : 31), false>(l16, mkrs(wp, NF * NF * 2), PTL == 1 ? bid * 128 : rr * 640); }
    if constexpr (FT > 0) {
      if (w == 7) {
        if constexpr (FT & 1) touch<1, true>(l16, rs_ws, WS_F1 + (bid & 63) * 128);
        if constexpr (FT & 2) touch<0xffffff, true>(l16, rs_ws, WS_LIST + bid * 3072);
        if constexpr (FT & 4) { touch<1, true>(l16, rs_ws, WS_LX + bid * 128); touch<1, true>(l16, rs_ws, WS_WT + bid * 128); }
        if constexpr (FT & 16) touch<0x1ff, false>(l16 << 14, rs_ws, (1 << 20) - 16384 + rr * 128);
        if constexpr (FT & 8) { touch<1, false>(l16, mkrs(b1, NH * 2), xl * 128); touch<1, false>(l16, mkrs(w2, NH * 2), (rr & 7) * 128); }
      }
    }
    if constexpr (FR) {
      asm volatile("s_cmp_lg_u32 %[w], 3\n\ts_cbranch_scc1 .Lb%=\n\ts_mov_b32 m0, %3\n\ts_nop 0\n\tbuffer_load_dwordx4 %2, %4, %5 offen lds\n\t"
                   "buffer_load_dword %0, %6, %7, %8 offen offset:384\n\tbuffer_load_dword %1, %6, %7, %8 offen offset:1984\n\t" FRB "s_branch .Le%=\n"
                   ".Lb%=:\n\t" FRB "s_mov_b32 m0, %3\n\ts_nop 0\n\tbuffer_load_dwordx4 %2, %4, %5 offen lds\n.Le%=:"
                   : "=&v"(xk), "=&v"(xh) : "v"(wo), "s"(wm), "s"(rs_w1), "s"(wso), "v"(pa), "s"(rs_idx), "s"(ps), [w] "s"(w) : "scc", "memory");
    }
    if constexpr (NB4 > 0) { if (w >= WB4) sfor<0, NB4>(req_t); }
    if constexpr (!FR) dma16<0, (NT & 1) != 0>(wo, wm, rs_w1, wso);
    auto touch2 = [&](int l128) {
      asm volatile("s_cmp_lg_u32 %[w], %[tw]\n\ts_cbranch_scc1 .Lnt%=\n\tbuffer_load_dword %0, %1, %2, %3 offen\n.Lnt%=:"
                   : "+v"(tt) : "v"(l128), "s"(rs_tab), "s"(rr * 8192), [w] "s"(w), [tw] "i"(T2W) : "scc");
    };
    if constexpr (T2W >= 0 && T2P == 0) touch2(l16);
    if constexpr (LA == 2) req_i();
    sj_t sj = 0;
    {
      const u64 sb = arg[3] + (unsigned)(phi * 24 * 128 * 1024 + xl * 128 + ((w >> 1) * 128 + (rr & 7) * 16 + (w & 1) * 8) * 1024);
      auto st8 = [&](u64 b) { stouch<0, 8>(sj, b); };
      if constexpr (ST0 & 1) st8(sb);
      if constexpr (ST0 & 4) {
        const u64 xb = arg[3] + (unsigned)(((96 + phi) * 128 + (rr & 7) * 16 + w * 2) * 1024 + xl * 128);
        stouch<0, 2>(sj, xb);
      }
      if constexpr (ST0 & 2) st8(sb + 512 * 1024);
      if constexpr (ST0 & 8) st8(sb + 1024 * 1024);
    }
    SCHED();
    const int lane = fresh_lane(), c = lane & 15, q = lane >> 4;
    if (w >= 6) dma16<0, (NT & 2) != 0>((lane ^ (par * 16)) * 1024, L_WX + (w - 6) * 1024, rs_w1, ((96 + phi) * 128 + (w - 6) * 64) * 1024 + (g ^ GX) * 16);
    if constexpr (T2W >= 0 && T2P == 1) touch2(l16);
    tag = *(const __attribute__((address_space(4))) u64*)(arg[7] + WS_EPOCH) * 6364136223846793005ull + 1442695040888963409ull | 1;
    l16 = lane * 16;
    if constexpr (IS & 2) { if (w == 5) BST128C(((u32x4){0u, 0u, 0u, 0u}), rs_ws, l16, WS_F2 + bid * 1024); }
    asm volatile("" ::: "memory");
    if constexpr (NB4 == 0) {
      const int h = c ^ (q << 2);
      dv[0] = q * 256 + (h ^ ((h & 4) << 1)) * 16;
      dv[1] = dv[0] ^ 16; dv[2] = dv[0] ^ 32; dv[3] = dv[0] ^ 48;
    }
    if (w == 3) {
      if constexpr (FR) { k0 = 0; half = 0; } else
      asm volatile("buffer_load_dword %0, %2, %3, %4 offen offset:384" NTX "\n\tbuffer_load_dword %1, %2, %3, %4 offen offset:1984" NTX
                   : "=&v"(k0), "=&v"(half) : "v"(((lane & 4) * 2 + (lane & 3)) * (NF * 4) + ((lane >> 3) & 3) * 4), "s"(rs_idx), "s"((128 * par + 8 * (bid >> 4) + xl) * 16 * NF * 4));
      const int id = bid * 23 + lane, tile = id >> 8, l = (id >> 2) & 63, r = id & 3, c = l & 15, q = l >> 4;
      int tj = (tile >= 1) + (tile >= 3) + (tile >= 6) + (tile >= 10) + (tile >= 15);
      const int ti = tile - tj * (tj + 1) / 2;
      int i = ti < 4 ? 16 * q + 4 * r + ti : 8 * q + 2 * r + ti + 60;
      bool oka = true, okb = ti != tj;
      if (tile >= 21) {
        tj = (tile - 21) * 4 + r; i = 96 + q;
        oka = tj < 6 || (tj == 6 && (c & 3) == 0); okb = tj < 6;
      }
      const int j = tj < 4 ? 4 * c + tj : tj < 6 ? 2 * c + tj + 60 : 96 + (c >> 2);
      asm volatile("buffer_load_ushort %0, %2, %4, 0 offen\n\tbuffer_load_ushort %1, %3, %4, 0 offen"
                   : "=&v"(c2), "=&v"(base) : "v"((oka && lane < 23) ? (i * NF + j) * 2 : 0x100000), "v"((okb && lane < 23) ? (j * NF + i) * 2 : 0x100000), "s"(mkrs(wp, NF * NF * 2)));
      if constexpr (IS & 4) { BST128C(((u32x4){0u, 0u, 0u, 0u}), rs_ws, l16, WS_F2 + bid * 1024); asm volatile("" ::: "memory"); }
      lso = lane >= 32 ? WS_LX - 32768 + (lane >> 3) * 8192 + (lane & 4) * 128 + (lane & 3) * 16 + (par * 4 + (bid >> 6)) * 1024 + (bid & 7) * 64 + ((~bid >> 4 ^ lane >> 3) & 3) * 4
                       : lane < 23 ? WS_WT + bid * 92 + lane * 4 : 0x7f000000;
    } else {
      k0 = (lane & 1) * 512 * 0x10001u + 0x10000u; half = (lane & 1) ? 0xffff0000u : 0xffffu; c2 = lane & 6;
      base = __builtin_amdgcn_perm(c2, c2, 0) ^ 0x03020100u;
      lso = (lane & 1) * 32768 + (lane & 32) * 16 + (x0 ^ ((lane & 16) ? 5 : 0)) * 64 + c2 * 8;
    }
    if (B0 & 1) asm volatile("s_barrier" ::: "memory");
    if constexpr (NB4 > 0) { if (w >= 3 && w < WB4) sfor<0, NB4>(req_t); }
    if constexpr (AB > 0) {
      if (w >= 4) {
        const unsigned dx = dv[0] ^ ((w & 3) * 16);
        sfor<0, 3 * AB>([&](auto N) { dma16<0>(dx, L_P + (N.v % 3) * 16384 + tslot(N.v / 3) + (w & 3) * 1024, rs_tab, (N.v % 3) * 32768 + (N.v / 3) * 4096 + (w & 3) * 1024); });
      }
    }
    if (LA || w >= 3) sfor<NB4, NPRE>(req_t);
    SCHED();
    if (w == 2 && lane < 2) *(u32x4*)(smem + L_MISC + lane * 16) = (u32x4){0u, 0u, 0u, 0u};
    toff = (c >> 2) * 256 + q * 64 + ((lane & 3) ^ (par * 2)) * 16;
    set_wad();
    const int pc = (c & 3) * 4 + (c >> 2);
    int ra[4], pw = slice + lane * 4, xw = L_PX + (128 * w + pc) * 16 + (q & 1) * 8, xa = L_WX + q * 512 + (c >> 2) * 16 + (c & 1) * 8;
    ra[0] = slice + pc * 256 + (((q ^ ((q & 1) << 1)) << 2) ^ c ^ (par * 2)) * 16;
    ra[1] = ra[0] ^ 16; ra[2] = ra[0] ^ 32; ra[3] = ra[0] ^ 48;
    asm volatile("" : "+v"(ra[0]), "+v"(ra[1]), "+v"(ra[2]), "+v"(ra[3]), "+v"(pw), "+v"(xw), "+v"(xa), "+v"(toff), "+v"(lso));
    auto read_t = [&](int o) { T[o >> 2][o & 3] = *(const u32x4*)(smem + ra[o & 3] + tslot(o >> 2)); };
    if (w < 3) vmwait<(LA == 0 ? 1 : LA == 1 ? NPRE : NPRE + 2)>(); else if (w == 3) vmwait<NPRE + 4 + ((IS & 4) != 0)>(); else if (NB4 > 0 && w >= WB4) vmwait<NPRE - NB4>(); else vmwait<NPRE>();
    asm volatile("" : "+v"(pi[0]), "+v"(pi[1]), "+v"(k0), "+v"(half), "+v"(c2), "+v"(base) : "v"(tt), "v"(tc));
    auto lists = [&]() __attribute__((always_inline)) {
      unsigned lo[2], hi[2], m[2];
#pragma unroll
      for (int e = 0; e < 2; ++e) {
        unsigned a = pi[e][0] | pi[e][1] << 16, b = pi[e][2] | pi[e][3] << 16, x, y;
        opaque(a); opaque(b);
        lo[e] = ((a + (a & 0x03800380u)) << 2) + k0; hi[e] = ((b + (b & 0x03800380u)) << 2) + (k0 + 0x20002u);
        asm("v_pk_lshlrev_b16 %0, %1, 1 op_sel_hi:[1,0]" : "=v"(x) : "v"(lo[e]));
        asm("v_pk_lshlrev_b16 %0, %1, 1 op_sel_hi:[1,0]" : "=v"(y) : "v"(hi[e]));
        x |= y; opaque(x);
        m[e] = (x | __builtin_amdgcn_alignbit(x, x, 16)) & half;
      }
      m[1] |= __builtin_amdgcn_mov_dpp(m[1], 0xB1, 0xf, 0xf, true);
      unsigned one = m[0] | __builtin_amdgcn_mov_dpp(m[0], 0x128, 0xf, 0xf, true), two = m[0] & __builtin_amdgcn_mov_dpp(m[0], 0x128, 0xf, 0xf, true), ch = 0;
      u32x4 o;
      o[0] = __builtin_amdgcn_perm(hi[0], lo[0], base); o[2] = __builtin_amdgcn_perm(lo[0], hi[0], base);
      sfor<0, 8>([&](auto S) {
        const unsigned v = __builtin_amdgcn_mov_dpp(m[1], 0x150 + 2 * S.v, 0xf, 0xf, true);
        const unsigned key = ((__builtin_popcount(v & two) * 4 + __builtin_popcount(v & one)) << 3) + c2;
        unsigned k = umin_(key, __builtin_amdgcn_mov_dpp(key, 0x4E, 0xf, 0xf, true));
        k = umin_(k, __builtin_amdgcn_mov_dpp(k, 0x124, 0xf, 0xf, true));
        const unsigned won = k == key ? v : 0u;
        two |= one & won; one |= won & half;
        opaque(one); opaque(two);
        ch = __builtin_amdgcn_alignbit(k, ch, 4);
      });
      const unsigned k = __builtin_amdgcn_ubfe(ch, (lane & 14) * 2, 3), sel = __builtin_amdgcn_perm(k, k, 0) ^ 0x03020100u;
      o[1] = __builtin_amdgcn_perm(hi[1], lo[1], sel); o[3] = __builtin_amdgcn_perm(lo[1], hi[1], sel);
#pragma unroll
      for (int e = 0; e < 2; ++e) { const auto t = __builtin_amdgcn_permlane32_swap(o[e], o[e + 2], false, false); o[e] = t[0]; o[e + 2] = t[1]; }
#pragma unroll
      for (int e = 0; e < 4; e += 2) { const auto t = __builtin_amdgcn_permlane16_swap(o[e], o[e + 1], false, false); o[e] = t[0]; o[e + 1] = t[1]; }
      BST128C(o, rs_ws, lso, WS_LIST + ((rr & 3) * 6 + 2 * w) * 32768 + (xl & 2) * 8192 + (lk ^ (~rr & 3)) * 1024);
    };
    if (!LA && w < 3) {
      lists();
      sfor<0, NPA>(req_t);
      vmwait<1 + NPA>();
    }
    asm volatile("" ::: "memory");
    if constexpr (SW) asm volatile("s_waitcnt lgkmcnt(0)" :: "{s95}"(sw) : "memory");
    else if (w < 4) asm volatile("s_waitcnt lgkmcnt(0)" ::: "memory");
    STAMP(1);
    asm volatile("s_barrier" ::: "memory");
    STAMP(2); CK_(0);
    if (!LA && w < 3) sfor<(AB ? 4 * AB : NPA), NPRE>(req_t);
    u32x4 Wx[4];
#pragma unroll
    for (int i = 0; i < 8; ++i) read_w(0, i);
#pragma unroll
    for (int i = 0; i < 4; ++i) Wx[i] = rdtr(smem + xa + i * 128, 64);
    SCHED();
    asm volatile("s_mov_b64 exec, 1\n\tds_add_u32 %0, %1 offset:4\n\ts_mov_b64 exec, -1" :: "v"(L_MISC), "v"(1) : "memory");
    if (PY0 && w >= 4) __builtin_amdgcn_s_setprio(1);
    if (w == 3) {
      asm volatile("s_waitcnt vmcnt(%4)" : "+v"(k0), "+v"(half), "+v"(c2), "+v"(base) : "i"(NPRE + ((IS & 4) != 0)));
      if constexpr (FR) { asm volatile("" : "+v"(xk), "+v"(xh)); k0 = xk; half = xh; }
      if constexpr (IS & 8) BST128C(((u32x4){0u, 0u, 0u, 0u}), rs_ws, l16, WS_F2 + bid * 1024);
      __builtin_amdgcn_raw_buffer_store_b32(lane < 32 ? __float_as_uint(bf2f(c2) + bf2f(base)) : k0 | (half << 16) | (L_PX / 16 * 0x10001u), rs_ws, lso, 0, SC1);
      asm volatile("" ::: "memory");
    }
    if constexpr (4 * AB > NPRE) { if (w >= 3) sfor<NPRE, 4 * AB>(req_t); }
    sfor<(4 * AB > NPRE ? 4 * AB : NPRE), NQ>(req_t);
    if (LA && w < 3) {
      asm volatile("s_waitcnt vmcnt(%2)" : "+v"(pi[0]), "+v"(pi[1]) : "i"(NQ));
      lists();
      asm volatile("" ::: "memory");
    }
    vmwait<behind(0, 0)>();
#pragma unroll
    for (int o = 0; o < 4; ++o) read_t(o);
    if (!LA && !AB && w < 3 && lane == 0) __hip_atomic_store((gu64*)(ws + WS_F1 + bid * 32 + w * 8), tag, RLX_AGENT);
    u32x4 fl[2];
    unsigned cnt;
    QT qt; qt_init(qt);
    f32x4 D[2][2], dx[2];
    RESERVE(D[0][0]); RESERVE(D[0][1]); RESERVE(D[1][0]); RESERVE(D[1][1]); RESERVE(dx[0]); RESERVE(dx[1]);
    unsigned pk[4];
    SCHED();
    auto block = [&](auto M, auto MM) {
      constexpr int m = M.v;
      constexpr bool mm = MM.v;
      sfor<0, 12>([&](auto K) {
        constexpr int k = K.v, t = 12 * m + k, o = rqat(t);
        if constexpr (mm && k < 8) mf<(k < 2)>(D[m & 1][k & 1], T[m][k >> 1], Wf[k & 1][k >> 1]);
        if constexpr (mm && k >= 8) mf<k == 8>(dx[m & 1], Wx[k - 8], T[m][k - 8]);
        if constexpr (!mm && k == 0) asm volatile("s_nop 4");
        if constexpr (!mm && k == 5) asm volatile("s_nop 1");
        SCHED();
        if constexpr (m < 7) {
          if constexpr (viareg(m + 1)) {
            if constexpr (k == 0) { vmwait2<behind(m + 1, t), others(rqt(4 * m + 7), t)>(w); asm volatile("" : "+v"(T[m + 1][0]), "+v"(T[m + 1][1]), "+v"(T[m + 1][2]), "+v"(T[m + 1][3])); }
            sfor<3 * k, (3 * k + 3 < 32 ? 3 * k + 3 : 32)>([&](auto N) { qt_step(T[m + 1], qt, N.v); });
          } else {
            if constexpr (k == KW) vmwait2<behind(m + 1, t), others(rqt(4 * m + 7), t)>(w);
            if constexpr (k >= KW && k < KW + 4) read_t(4 * m + 4 + k - KW);
          }
        }
        if constexpr ((LA || AB) && t == TPA) { if (w < 3) { vmwait<(AB ? NQ - 4 * AB : 0) + since(-2, TPA)>(); if (l16 == 0) __hip_atomic_store((gu64*)(ws + WS_F1 + bid * 32 + w * 8), tag, RLX_AGENT); } }
        if constexpr (t == TP) { if (w == 3) { vmwait<NQ - NPRE + since(-2, TP)>(); if (l16 == 0) __hip_atomic_store((gu64*)(ws + WS_F1 + bid * 32 + 24), tag, RLX_AGENT); } }
        if constexpr (o >= 0) req_t(IC<o>{});
        if constexpr (t == TC - 4) cnt = __hip_atomic_load((unsigned*)(smem + L_MISC + 4), __ATOMIC_RELAXED, __HIP_MEMORY_SCOPE_WORKGROUP);
        if constexpr (t == TC) {
          if (w < 4) {
#pragma unroll 1
            for (int spins = 0; spins < (1 << 20) && __builtin_amdgcn_readfirstlane(cnt) != 8u; ++spins)
              cnt = __hip_atomic_load((unsigned*)(smem + L_MISC + 4), __ATOMIC_RELAXED, __HIP_MEMORY_SCOPE_WORKGROUP);
          }
        }
        if constexpr (t == TFL) {
          ld8<0>(bz, (l16 >> 5) & 8, mkrs(b1, phi == 0 ? NH * 2 : 0), g * 16);
          asm volatile("s_cmp_gt_u32 %5, 3\n\ts_cbranch_scc1 .Lnf%=\n\tbuffer_load_dwordx4 %0, %2, %3, %4 offen sc1\n\tbuffer_load_dwordx4 %1, %2, %3, %4 offen offset:1024 sc1\n.Lnf%=:"
                       : "=&v"(fl[0]), "=&v"(fl[1]) : "v"(l16), "s"(rs_ws), "s"(WS_F1 + w * 2048), "s"(w) : "memory", "scc");
        }
        if constexpr (t == TCK) {
          vmwait2<since(TFL, TCK + 1), others(TFL + 1, TCK)>(w);
          asm volatile("" : "+v"(fl[0]), "+v"(fl[1]), "+v"(bz));
          if (w < 4) {
            const unsigned tl = (unsigned)tag, th = (unsigned)(tag >> 32);
            if (!__all(((fl[0][0] ^ tl) | (fl[0][1] ^ th) | (fl[0][2] ^ tl) | (fl[0][3] ^ th) | (fl[1][0] ^ tl) | (fl[1][1] ^ th) | (fl[1][2] ^ tl) | (fl[1][3] ^ th)) == 0u)) {
#pragma unroll 1
              for (int i = 0; i < 4; ++i) wait_flags(ws + WS_F1 + w * 2048 + i * 512, tag, 8, 63);
            }
            __builtin_amdgcn_fence(__ATOMIC_ACQUIRE, "wavefront");
          }
          asm volatile("s_cmp_gt_u32 %9, 3\n\ts_cbranch_scc1 .Lnx%=\n\ts_mov_b32 m0, %10\n\tbuffer_load_dwordx4 %0, %5, %6, %8 offen sc1\n\tbuffer_load_dwordx4 %5, %6, %11 offen sc1 lds\n\t"
                       "buffer_load_dwordx4 %1, %5, %6, %7 offen sc1\n\tbuffer_load_dwordx4 %2, %5, %6, %7 offen offset:1024 sc1\n\t"
                       "buffer_load_dwordx4 %3, %5, %6, %7 offen offset:2048 sc1\n\tbuffer_load_dwordx4 %4, %5, %6, %7 offen offset:3072 sc1\n.Lnx%=:"
                       : "=&{v[16:19]}"(lx), "=&{v[0:3]}"(ix[0]), "=&{v[4:7]}"(ix[1]), "=&{v[8:11]}"(ix[2]), "=&{v[12:15]}"(ix[3])
                       : "v"(l16), "s"(rs_ws), "s"(lsb), "s"(WS_LX + phi * 8192 + w * 1024), "s"(w), "s"(L_WB + w * 1024), "s"(WS_LX + phi * 8192 + w * 1024 + 4096) : "memory", "scc");
        }
        if constexpr (t == TT) { if (w < 4) touches(); }
        if constexpr (t == T7) { if (w == 7) { touch<0xffffffff, false>(l16 << 6, rs_w1, wsb + (1024 + (rr & 7) * 64) * 1024); touch<0xffffffff, false>(l16 << 6, rs_w1, wsb + (1056 + (rr & 7) * 64) * 1024); } }
        if constexpr (t == TW) { if (w < 4) { dma_w(1, l16, wv); dma_w(1, l16, wv ^ 4); } }
        if constexpr (m > 0 && k >= 1 && k < 5) pk[k - 1] = pack2(D[(m - 1) & 1][0][k - 1], D[(m - 1) & 1][1][k - 1]);
        if constexpr (m > 0 && (k == 6 || k == 7)) {
          *(unsigned*)(smem + pw + (m - 1) * 1024 + (2 * k - 12) * 256) = pk[2 * k - 12];
          *(unsigned*)(smem + pw + (m - 1) * 1024 + (2 * k - 11) * 256) = pk[2 * k - 11];
        }
        if constexpr (m > 0 && k == 5) {
          const f32x4 d = dx[(m - 1) & 1];
          *(u32x2*)(smem + xw + (m - 1) * 256) = (u32x2){pack2(d[0], d[1]), pack2(d[2], d[3])};
        }
        SCHED();
      });
    };
    sfor<0, 7>([&](auto M) { block(M, IC<1>{}); });
    if (w < 7) { block(IC<7>{}, IC<1>{}); block(IC<8>{}, IC<0>{}); }
    else block(IC<7>{}, IC<0>{});
  }

  f32x4 acc[16];
  constexpr int NG = 2 * ((RD + 1) / 2);
  u32x4 gv[NG];
  unsigned ea[4][7];
  auto load_ea = [&]() {
    renew();
    const int so = ((bid & ~8) * 16 + w) * NF * 4, vo = ((l16 >> 8) & 1) * (8 * NF * 4) + (((l16 >> 9) ^ par ^ 1) & 1) * (128 * NF * 4);
    const u32x4 a = BLD128(rs_idx, vo + (l16 & 240), so);
    const u32x2 b = __builtin_amdgcn_raw_buffer_load_b64(rs_idx, vo + ((l16 >> 1) & 120) + 256, so, 0);
    ea[0][0] = a[0]; ea[0][1] = a[1]; ea[0][2] = a[2]; ea[0][3] = a[3]; ea[0][4] = b[0]; ea[0][5] = b[1];
    ea[0][6] = __builtin_amdgcn_raw_buffer_load_b32(rs_idx, vo + ((l16 >> 4) & 12) + 384, so, 0);
  };
  auto rd = [&](int n) { gv[n % NG] = *(const u32x4*)(smem + L_P + half16(ix[n >> 3][(n >> 1) & 3], n & 1)); };
  auto requests = [&](auto J) {
    constexpr int j = J.v;
    renew();
    if constexpr (j < 5) dma_w(j + 1, l16, wv);
    if constexpr (j >= 3) {
      const int tile = j == 3 ? w : j == 4 ? (w < 7 ? 8 + w : 0) : 15 + w;
      dma16c(l16, L_WT + tile * 1024, rs_ws, WS_WT + tile * 1024);
    }
  };
  auto extra = [&](auto OLD) __attribute__((always_inline)) {
    constexpr bool old = OLD.v;
    constexpr int XR = old ? XRB : XRA;
    u32x4 xv[8];
    renew();
    if constexpr (old) {
      int la = l16 + L_WB + (w - 4) * 1024; opaque(la);
      STAMP(3);
      BAR();
      S1(20);
      __builtin_amdgcn_s_setprio(PXB);
      lx = *(const u32x4*)(smem + la);
      SCHED();
      load_ix(0, 0, 2);
      set_wad();
    }
    SCHED();
    f32x4 a0 = {bf2f(bz[0]), bfhi(bz[0]), bf2f(bz[1]), bfhi(bz[1])};
    opaque(a0);
    const int q = l16 >> 8;
    const u32x4 sd = make_sel((l16 >> 4) & 15, q);
    u32x4 slo = q < 2 ? sd : (u32x4){0, 0, 0, 0}, shi = q < 2 ? (u32x4){0, 0, 0, 0} : sd;
    opaque(slo); opaque(shi);
    SCHED();
    if constexpr (!old) {
      asm volatile("s_waitcnt vmcnt(%1)" : "+{v[16:19]}"(lx) : "i"(4 + since(TCK, 99) + others(TCK + 1, 99)) : "memory");
      STAMP(3);
      BAR();
      S1(20);
      __builtin_amdgcn_s_setprio(PXA);
    }
    auto rx = [&](int n) { if (n < 8) xv[n & 7] = *(const u32x4*)(smem + half16(lx[(n >> 1) & 3], n & 1)); };
#pragma unroll
    for (int n = 0; n < XR; ++n) rx(n);
    SCHED();
    S1(22);
    sfor<0, 8>([&](auto N) {
      constexpr int n = N.v, a = 2 * (n & 6) + (n & 1), at[7] = {56, 58, 57, 59, 60, 61, 63};
      if constexpr (n < 7) pinq<at[n]>(xv[n]);
      acc_init<a>(acc[a], slo, xv[n], a0);
      SCHED();
      rx(n + XR);
      SCHED();
      acc_init<a + 2>(acc[a + 2], shi, xv[n], a0);
      SCHED();
    });
    S1(6);
    asm volatile("s_nop 2"); use(a0);
    if constexpr (!old) {
      if constexpr (TN == -1) touches();
      asm volatile("s_waitcnt vmcnt(%4)" : "+{v[0:3]}"(ix[0]), "+{v[4:7]}"(ix[1]), "+{v[8:11]}"(ix[2]), "+{v[12:15]}"(ix[3]) : "i"((T7 < 0 ? 3 : 2) * (TT > TW || TN == -1)) : "memory");
#pragma unroll
      for (int n = 0; n < XP; ++n) rd(n);
      SCHED();
    }
    asm volatile("s_barrier" ::: "memory");
    if constexpr (!old) {
#pragma unroll
      for (int n = XP; n < RD; ++n) rd(n);
    }
    S1(21);
  };
  auto make_sp = [&](u32x4& sel, unsigned& si) {
    renew();
    const int c = (l16 >> 4) & 15, gi = ((c >> 2) & 2) + ((c >> 1) & 1);
#pragma unroll
    for (int k = 0; k < 4; ++k) sel[k] = gi == k ? 0x3F80u : 0u;
    si = ((c & 1) * 2 + ((c >> 2) & 1)) * 0x5555u;
    asm volatile("s_nop 1" : "+v"(sel), "+v"(si));
  };
  {
    sfor<0, 8>([&](auto M) { pin4<M.v>(T[M.v]); });
    asm volatile("" : "+{v[16:19]}"(lx), "+{v[0:3]}"(ix[0]), "+{v[4:7]}"(ix[1]), "+{v[8:11]}"(ix[2]), "+{v[12:15]}"(ix[3]));
  }
  auto arrive = [&]() { unsigned a, b; asm volatile("s_mov_b64 exec, 1\n\tv_mov_b32 %0, %2\n\tv_mov_b32 %1, 1\n\tds_add_u32 %0, %1 offset:8\n\ts_mov_b64 exec, -1" : "=&v"(a), "=&v"(b) : "i"(L_MISC) : "memory"); };
  auto fill = [&](auto OLD, auto J) __attribute__((always_inline)) {
    constexpr bool old = OLD.v;
    constexpr int j = J.v, jn = old ? j - 1 : j;
    sfor<0, 8>([&](auto M) { pin4<M.v>(T[M.v]); });
    constexpr int prc = old ? PRCB : PRC;
    if constexpr (old) {
      if constexpr (j == 1 || !PRE) {
        __builtin_amdgcn_s_setprio(PRW);
#pragma unroll
        for (int i = 0; i < 8; ++i) read_w(j, i);
        SCHED();
      }
      if constexpr (j == 1 && UIX) { use(ix[0]); use(ix[1]); }
      if constexpr (j > 1 && IB0 < 0) { load_ix(jn, 0, NI0); SCHED(); }
      if constexpr (RQU < 0 || j == 1) requests(J);
    }
    __builtin_amdgcn_s_setprio(prc);
    set_m0((w & 3) * 16384 + (j & 1) * 8192 + (old ? 8188 : 0));
    f32x4 D[2][2];
    RESERVE(D[0][0]); RESERVE(D[0][1]); RESERVE(D[1][0]); RESERVE(D[1][1]);
    unsigned pk[4];
    SCHED();
    sfor<0, 9>([&](auto U) {
      constexpr int u = U.v;
      sfor<0, 8>([&](auto K) {
        constexpr int k = K.v, s = k >> 1, t = k & 1;
        if constexpr (u < 8) mf<s == 0>(D[u & 1][t], T[u][s], Wf[t][s]);
        SCHED();
        if constexpr (u == 8 && k == 0) {
          if constexpr (old && GPO) {
#pragma unroll
            for (int n = ERD; n < RD; ++n) rd(n);
            if constexpr (RD - ERD < 4) asm volatile("s_nop 7");
          } else asm volatile("s_nop 7");
        }
        if constexpr (old && GPO && u == 7 && k >= EK0 && k - EK0 < ERD) rd(k - EK0);
        if constexpr (j == 1 && u == 0 && k == 7) S1(4);
        if constexpr (u == 1 && k == 3) { asm volatile("s_nop 3"); SCHED(); }
        if constexpr (u >= 1 && k >= 3 && k < 7) pk[k - 3] = pack2(D[(u - 1) & 1][0][k - 3], D[(u - 1) & 1][1][k - 3]);
        if constexpr (u >= 1 && k == 7) {
          __builtin_amdgcn_s_setprio(PRW);
          sfor<0, 4>([&](auto R) { wat<(old ? 57348 : 0) + (u - 1) * 1024 + R.v * 256>(pk[R.v]); });
          if constexpr (u < 8) __builtin_amdgcn_s_setprio(prc);
        }
        if constexpr (PRE && !old && j < 5 && u == UW && k == 0) { vmwait<(j < 4) + (j >= 3) + (j == WTJ) + 2 * (UW > 4)>(); arrive(); }
        if constexpr (old && j > 1) {
          if constexpr (u == IB0 && k == 1) load_ix(jn, 0, NI0);
          if constexpr (u == RQU && k == 2) { requests(J); set_m0((w & 3) * 16384 + (j & 1) * 8192 + 8188); }
          if constexpr (u == IB1 && k == 7) load_ix(jn, NI0, 4);
        } else {
          if constexpr (u == 4 && k == 1 && jn > 0) load_ix(jn, 0, 2);
          if constexpr (u == 7 && k == 7) load_ix(jn, 2, 4);
        }
        if constexpr (!old && j < 4 && u == SDU && k == 2) { renew(); touch<0xffff, false>(l16 << 6, rs_w1, wsb + ((j + 2) * 512 + (rr & 7) * 64 + (w & 3) * 16) * 1024); set_m0((w & 3) * 16384 + (j & 1) * 8192); }
        if constexpr (!old && j == WTJ && u == SDU && k == 4) { renew(); touch<0x1ffff, false>(l16 << 16, rs_ws, (1 << 19) + 0x60000 + (w & 3) * 4096 + rr * 128); set_m0((w & 3) * 16384 + (j & 1) * 8192); }
        if constexpr (!old && j == 5 && u == 2 && k == 1) load_ea();
        SCHED();
      });
    });
  };
  auto gather = [&](auto OLD, auto J) __attribute__((always_inline)) {
    constexpr bool old = OLD.v;
    constexpr int j = J.v;
    __builtin_amdgcn_s_setprio(old ? PRGB : PRG);
    if constexpr (old ? !GPO : j > 0) {
#pragma unroll
      for (int n = 0; n < RD; ++n) rd(n);
    }
    SCHED();
    u32x4 sel; unsigned si, cnt;
    make_sp(sel, si);
    SCHED();
    sfor<0, 16>([&](auto N) {
      constexpr int n = N.v;
      acc_add<n>(acc[n], sel, gv[(2 * n) % NG], gv[(2 * n + 1) % NG], si);
      SCHED();
      if constexpr (2 * n + RD < 32) rd(2 * n + RD);
      if constexpr (2 * n + RD + 1 < 32) rd(2 * n + RD + 1);
      if constexpr (!old && j == 0 && n == TN) touches();
      if constexpr (n == SDN) {
        if constexpr (!old) {
          requests(IC<j + 1>{});
        }
      }
      if constexpr (!old && j < 3 && n == SDT && SDU < 0) { if constexpr (SDT != SDN) renew(); touch<0xffff, false>(l16 << 6, rs_w1, wsb + ((j + 3) * 512 + (rr & 7) * 64 + (w & 3) * 16) * 1024); }
      if constexpr (YC && j == YCJ && n == YCN) {
        renew();
        u32x4 z = {0u, 0u, 0u, 0u}; opaque(z);
        if constexpr (YC & 1) BST128C(z, rs_ws, (l16 >> 7) * YST + (l16 & 112), WS_Y + (32 * w + 8 * phi) * YST + g * 128);
        if constexpr (YC & 2) BST128C(z, rs_ws, l16, WS_PN + ((bid ^ 8) * 8 + w) * 1024);
        asm volatile("" ::: "memory");
      }
      if constexpr (PRE && old && j < 4) {
        if constexpr (n == NAR) { vmwait<(j == 0 ? 2 : 4 - NI0)>(); arrive(); }
        if constexpr (n == WNB - NCL) cnt = __hip_atomic_load((unsigned*)(smem + L_MISC + 8), __ATOMIC_RELAXED, __HIP_MEMORY_SCOPE_WORKGROUP);
        if constexpr (n == WNB - 1) set_wad();
        if constexpr (n == WNB) {
#pragma unroll 1
          for (int spins = 0; spins < (1 << 20) && __builtin_amdgcn_readfirstlane(cnt) != 8u * (j + 1); ++spins)
            cnt = __hip_atomic_load((unsigned*)(smem + L_MISC + 8), __ATOMIC_RELAXED, __HIP_MEMORY_SCOPE_WORKGROUP);
          asm volatile("" ::: "memory");
        }
        if constexpr (n >= WNB) sfor<(n - WNB) * 8 / (16 - WNB), (n + 1 - WNB) * 8 / (16 - WNB)>([&](auto I) { read_w(j + 2, I.v); });
      } else if constexpr (n == WN0 - 1 && !(old && j == 4)) set_wad();
      if constexpr (old && j == 4 && n == 4) load_ea();
      if constexpr (old && j == 4 && n >= 12) load_ix(5, n - 12, n - 11);
      if constexpr (!old && n >= WN0) { read_w(j + 1, 2 * (n - WN0)); read_w(j + 1, 2 * (n - WN0) + 1); }
      SCHED();
    });
  };
  auto period = [&](auto OLD, auto J) __attribute__((always_inline)) {
    constexpr bool old = OLD.v;
    constexpr int j = J.v;
    if constexpr (j == 1) extra(OLD);
    if constexpr (old) fill(OLD, J); else gather(OLD, IC<j - 1>{});
    if constexpr (j == 1) STAMP(5);
    if constexpr (j == 3) STAMP(8);
    if constexpr (j == 5) STAMP(14);
    if constexpr (old) gather(OLD, IC<j - 1>{}); else fill(OLD, J);
    if constexpr (j < 5) {
      vmwait<(old ? (j == YCJ + 1) * ((YC & 1) + (YC >> 1)) : 4)>();
      if constexpr (j == 3) TSTAMP(9);
      if constexpr (j == 1) S1(9);
      if constexpr (old && PRE && NBW) asm volatile("s_barrier" ::: "memory"); else BAR();
      STAMP(j == 1 ? 19 : j == 2 ? 7 : j == 3 ? 10 : 11);
      if constexpr (j == 1) { S1END(); CK_(1); }
      if constexpr (j == 4) SFEND();
    }
  };

  auto rest = [&](auto OLD) __attribute__((always_inline)) {
    constexpr bool old = OLD.v;
    constexpr int CS = old ? CSB : CSA, NC = old ? NCB : NCA, KF = old ? KFB : KFA, KX = old ? KXB : KXA, KG = old ? KGB : KGA;
    u32x4 xs[6];
    int tw, q16, wa;
    constexpr int R5 = old ? RDB : RD5, N5 = 2 * ((R5 + 1) / 2), t0 = old ? TWB : TW0;
    u32x4 g5[N5];
    auto rd5 = [&](int n) { g5[n % N5] = *(const u32x4*)(smem + L_P + half16(ix[n >> 3][(n >> 1) & 3], n & 1)); };
    auto xsend = [&](auto T_) {
      sfor<0, 6>([&](auto C) {
        constexpr int ch = C.v;
        if constexpr ((old ? XB[ch] : XT[ch]) == T_.v) { SCHED(); BST128C(xs[ch], rs_ws, wa, WS_X - L_WT + ((bid ^ (3 - ch / 2) * 64) * 8 + w) * 6144 + ch * 1024); }
      });
    };
    auto publish = [&](auto K) {
      if constexpr (K.v == (old ? KPB : KP)) {
        const int lane = fresh_lane(), fo = lane < 3 ? ((bid * 1024) ^ ((3 - lane) * 65536)) + lane * 8 : 0x7f000000;
        const u32x2 tg = {(unsigned)tag, (unsigned)(tag >> 32)};
        hold(fo); hold(tg);
        DRAIN();
        __builtin_amdgcn_raw_buffer_store_b64(tg, rs_ws, fo, WS_F2 + w * 128, SC1);
        SCHED();
        TSTAMP(21); RST(2);
      }
    };
    f32x4 wt[23];
    auto read_wt = [&](auto U) { if constexpr (wnew(U.v)) wt[wti(U.v)] = *(const f32x4*)(smem + wa + wti(U.v) * 1024); };
    auto spread = [&](int s, int b) {
      const auto h = __builtin_amdgcn_permlane16_swap(ea[s][b], ea[s][b], false, false);
      ea[s][b] = h[0] ^ q16; ea[s + 1][b] = h[1] ^ q16;
      asm volatile("" : "+v"(ea[s][b]), "+v"(ea[s + 1][b]));
    };
    auto place = [&](int b) {
      const unsigned v = ZR && b == 6 ? pick(ea[0][b], 1008, 0xEEEEEEEEEEEEEEEEull) : ea[0][b], a = ((v + (v & 0x380u)) << 6) | ((v << 2) & 48u);
      const auto h = __builtin_amdgcn_permlane32_swap(a, a, false, false);
      ea[0][b] = h[0]; ea[2][b] = h[1];
      opaque(ea[2][b]); spread(0, b);
    };
    {
      u32x4 sel; unsigned si;
      make_sp(sel, si);
      const int lane = fresh_lane(), c = lane & 15, q = lane >> 4;
      tw = slice + ((c & 3) * 4 + (c >> 2)) * 64 + (q ^ (c & 3)) * 16;
      q16 = lane & 48;
      wa = L_WT + lane * 16;
      opaque(tw); opaque(q16); opaque(wa);
      SCHED();
      if constexpr (!old) vmwait<7>();
      BAR();
      STAMP(12); RST(1); CK_(2);
      __builtin_amdgcn_s_setprio(old ? POG : PYG);
#pragma unroll
      for (int m = 0; m < 8; ++m) if (t0 + m * TWS < 0) *(u32x4*)(smem + tw + m * 1024) = T[m][0];
      SCHED();
#pragma unroll
      for (int n = 0; n < R5; ++n) rd5(n);
      SCHED();
      sfor<0, 17>([&](auto N) {
        constexpr int n = N.v;
        if constexpr (n < 16) acc_add<n>(acc[n], sel, g5[(2 * n) % N5], g5[(2 * n + 1) % N5], si);
        SCHED();
        if constexpr (2 * n + R5 < 32) rd5(2 * n + R5);
        if constexpr (2 * n + R5 + 1 < 32) rd5(2 * n + R5 + 1);
        if constexpr (n >= t0 && (n - t0) % TWS == 0 && (n - t0) / TWS < 8) *(u32x4*)(smem + tw + (n - t0) / TWS * 1024) = T[(n - t0) / TWS][0];
        if constexpr (AC0 <= n && (n - AC0) % ACS == 0 && (n - AC0) / ACS < 7) place((n - AC0) / ACS);
        if constexpr (n >= 2 && n < 14 && (n & 1) == 0) {
          asm volatile("s_nop 3");
          constexpr int ch = (n >> 1) - 1;
          SCHED();
          const f32x4 a = acc[2 * ch], b = acc[2 * ch + 1];
          xs[ch] = (u32x4){pack2(a[0], a[1]), pack2(a[2], a[3]), pack2(b[0], b[1]), pack2(b[2], b[3])};
          opaque(xs[ch]);
        }
        xsend(N);
        SCHED();
        if constexpr (n == 16) {
          TSTAMP(4);
#pragma unroll
          for (int m = 0; m < 8; ++m) if (t0 + m * TWS > 16) *(u32x4*)(smem + tw + m * 1024) = T[m][0];
          SCHED();
        }
      });
    }
    TSTAMP(13);
    publish(IC<-4>{});
    BAR();
    TSTAMP(15); RST(10);
    u32x4 E[7][2];
    f32x4 D[LAG + 1];
    float sm[4];
    auto read_e = [&](int s, int f) { E[eb(f)][f & 1] = *(const u32x4*)(smem + ea[s][eb(f)] + (f & 1) * 8192); };
    sfor<0, 8>([&](auto M) {
      constexpr int m = M.v;
      *(u32x4*)(smem + tw + m * 1024 + 8192) = T[m][1];
      SCHED();
      if constexpr (m < 7) sfor<m * NWI / 7, (m + 1) * NWI / 7>(read_wt);
      if constexpr (m < NEI) read_e(0, 2 * m);
      SCHED();
    });
    u32x4 X[8];
    sfor<0, 4>([&](auto J) {
      constexpr int j = J.v;
      acc_pin<12 + (j & 2)>(acc[12 + (j & 2)]); acc_pin<13 + (j & 2)>(acc[13 + (j & 2)]);
      const f32x4 a = acc[12 + (j & 2)], b = acc[13 + (j & 2)];
      X[2 * j + 1] = (u32x4){pack2(a[0], a[1]), pack2(a[2], a[3]), pack2(b[0], b[1]), pack2(b[2], b[3])};
      pinq<21 + 8 * j>(X[2 * j + 1]);
    });
    SCHED();
#pragma unroll
    for (int b = NEI; b < NE0; ++b) read_e(0, 2 * b);
    sfor<NWI, NW0>(read_wt);
    SCHED();
    xsend(IC<17>{});
    publish(IC<-3>{});
    asm volatile("s_waitcnt lgkmcnt(%0)\n\ts_barrier" :: "i"(NE0 - NEI + wcount(NWI, NW0)) : "memory");
    TSTAMP(20); CK_(3);
    xsend(IC<18>{});
    publish(IC<-2>{});
#pragma unroll
    for (int f = 0; f < 14; ++f) if (ER[f] < 0 && ((f & 1) || f >= 2 * NE0)) read_e(0, f);
    sfor<NW0, WL>(read_wt);
    sfor<0, LAG + 1>([&](auto I) { RESERVE4(D[I.v]); pinq<(I.v < 2 ? 62 + I.v : I.v == 2 ? 47 : 18 - I.v)>(D[I.v]); });
    SCHED();
    u32x2 f2, wp2;
    u32x4 msg, yv, sel;
    unsigned bb, mt[2];
    int yo, oo;
    f32x4 z[4];
    float s2[4];
    int fo, xo, tries = 1;
    const int xb = WS_X - L_WT + (bid * 8 + w) * 6144, fb = WS_F2 + (bid * 8 + w) * 128;
    const int mb = WS_PN - L_WT + (bid * 8 + w) * 1024, yb = WS_Y - 8 * L_WT + bid * YST + w * 16;
    auto side = [&](auto K) {
      constexpr int k = K.v;
      xsend(IC<k + 20>{});
      publish(K);
      if constexpr (k == (old ? KMB : KM)) {
        ld16c<0>(msg, wa, rs_ws, mb);
        asm volatile("buffer_load_short_d16_hi %0, off, %1, 0" : "+v"(bb) : "s"(mkrs((char*)(gp)arg[6], 2)));
        SCHED();
      }
      if constexpr (k == (old ? KYB : KY)) { ld16c<0>(yv, yo, rs_ws, yb); SCHED(); }
      if constexpr (k == KF) { fo = umin_(q16, 32) >> 1; asm volatile("buffer_load_dwordx2 %0, %1, %2, %3 offen sc1" : "=v"(f2) : "v"(fo), "s"(rs_ws), "s"(fb) : "memory"); SCHED(); }
      if constexpr (k >= KX && k <= KX + (NC - 1) * CS && (k - KX) % CS == 0) {
        if constexpr (k == KX) {
          RST(3);
          sfor<0, 4>([&](auto J) { RESERVE(X[2 * J.v]); pinq<17 + 8 * J.v>(X[2 * J.v]); });
          xo = wa + (q16 & 32) * 48;
        }
        if constexpr (k == KX + (NC - 1) * CS) tries <<= 16;
        int busy;
        asm volatile("s_getreg_b32 %[t], hwreg(HW_REG_IB_STS, 0, 4)\n\ts_cmp_lt_u32 %[t], %[n]\n\ts_cbranch_scc0 .Ldone%=\n"
                     ".Lagain%=:\n\ts_waitcnt vmcnt(0)\n\tv_cmp_ne_u64 vcc, %[tag], %[f]\n\ts_cmp_eq_u64 vcc, 0\n\ts_cbranch_scc1 .Lgo%=\n\t"
                     "buffer_load_dwordx2 %[f], %[fo], %[rs], %[fb] offen sc1\n\ts_cmp_eq_u32 %[n], 1\n\ts_cbranch_scc1 .Ldone%=\n\t"
                     "s_sleep %[slp]\n\ts_sub_u32 %[n], %[n], 1\n\ts_branch .Lagain%=\n"
                     ".Lgo%=:\n\t" GOST
                     "buffer_load_dwordx4 %0, %[xo], %[rs], %[xb] offen sc1\n\tbuffer_load_dwordx4 %2, %[xo], %[rs], %[xb] offen offset:512 sc1\n\t"
                     "buffer_load_dwordx4 %4, %[xo], %[rs], %[xb] offen offset:1024 sc1\n\tbuffer_load_dwordx4 %6, %[xo], %[rs], %[xb] offen offset:1536 sc1\n\t"
                     "s_mov_b32 exec_lo, 0\n\tbuffer_load_dwordx4 %1, %[xo], %[rs], %[xb] offen offset:2048 sc1\n\tbuffer_load_dwordx4 %5, %[xo], %[rs], %[xb] offen offset:3072 sc1\n\t"
                     "s_not_b64 exec, exec\n\tbuffer_load_dwordx4 %3, %[xo], %[rs], %[xc] offen offset:512 sc1\n\tbuffer_load_dwordx4 %7, %[xo], %[rs], %[xc] offen offset:1536 sc1\n\t"
                     "s_mov_b64 exec, -1\n\ts_mov_b32 %[n], 0\n"
                     ".Ldone%=:"
                     : "+v"(X[0]), "+v"(X[1]), "+v"(X[2]), "+v"(X[3]), "+v"(X[4]), "+v"(X[5]), "+v"(X[6]), "+v"(X[7]), [f] "+v"(f2), [n] "+s"(tries), [t] "=&s"(busy) GOSO
                     : [xo] "v"(xo), [fo] "v"(fo), [rs] "s"(rs_ws), [xb] "s"(xb), [xc] "s"(xb + 4096), [fb] "s"(fb), [tag] "s"(tag), [slp] "i"(SLP) : "memory", "scc", "vcc");
        SCHED();
      }
      if constexpr (k == KG - 30) ld8<0>(wp2, (q16 & 32) >> 2, mkrs(w2, NH * 2), g * 16);
      if constexpr (k == KG - 10) { sel = make_sel((wa >> 4) & 15, q16 >> 4); SCHED(); }
      if constexpr (k == KG - 9) {
        GSTAMP(5);
        asm volatile("s_waitcnt vmcnt(%9)" : "+v"(X[0]), "+v"(X[1]), "+v"(X[2]), "+v"(X[3]), "+v"(X[4]), "+v"(X[5]), "+v"(X[6]), "+v"(X[7]), "+v"(wp2) : "i"(KG - 30 < KS && KS <= k));
        GSTAMP(7);
      }
      if constexpr (k >= KG - 9 && k < KG - 1) {
        constexpr int j = (k - KG + 9) >> 1, s = (k - KG + 9) & 1;
        if constexpr (s == 0) { RESERVE4(z[j]); SCHED(); }
        mf<s == 0>(z[j], sel, X[2 * j + s]);
      }
      sfor<0, 4>([&](auto J) {
        constexpr int j = J.v;
        if constexpr (k == KG + j * GS) {
          constexpr float c1 = -2.f * 0.7978845608028654f * 0.044715f * 1.4426950408889634f, c0 = -2.f * 0.7978845608028654f * 1.4426950408889634f;
          const f32x4 ww = {bf2f(wp2[0]), bfhi(wp2[0]), bf2f(wp2[1]), bfhi(wp2[1])};
          float x[4], y[4];
#define EACH(st) _Pragma("unroll") for (int r = 0; r < 4; ++r) { st; }
          EACH(y[r] = z[j][r] * z[j][r]; opaque(y[r]))
          EACH(y[r] = __builtin_fmaf(y[r], c1, c0); opaque(y[r]))
          EACH(y[r] *= z[j][r]; opaque(y[r]))
          EACH(y[r] = __builtin_amdgcn_exp2f(y[r]); opaque(y[r]); x[r] = z[j][r] * ww[r]; opaque(x[r]))
          EACH(y[r] += 1.f; opaque(y[r]))
          EACH(y[r] = __builtin_amdgcn_rcpf(y[r]); opaque(y[r]))
          s2[j] = __builtin_fmaf(y[3], x[3], __builtin_fmaf(y[2], x[2], __builtin_fmaf(y[1], x[1], y[0] * x[0])));
          hold(s2[j]);
          if constexpr (j == 3) {
            const auto t0 = __builtin_amdgcn_permlane32_swap(__float_as_uint(s2[0]), __float_as_uint(s2[1]), false, false);
            const auto t1 = __builtin_amdgcn_permlane32_swap(__float_as_uint(s2[2]), __float_as_uint(s2[3]), false, false);
            const u32x4 m = {__float_as_uint(__uint_as_float(t0[0]) + __uint_as_float(t0[1])), (unsigned)tag, __float_as_uint(__uint_as_float(t1[0]) + __uint_as_float(t1[1])), (unsigned)(tag >> 32)};
            BST128C(m, rs_ws, (((q16 >> 3) & 4) + ((wa >> 6) & 3)) * YST + (q16 & 16) * 4 + (wa & 48), WS_Y + (32 * w + 8 * phi) * YST + g * 128);
            GSTAMP(8); RST(8);
          }
          SCHED();
        }
      });
      constexpr int pr = level(old, k);
      if constexpr (pr >= 0) __builtin_amdgcn_s_setprio(pr);
    };
    side(IC<-1>{});
    sfor<0, 2 * (112 + LAG)>([&](auto K) {
      constexpr int k = K.v, U = k >> 1, s = k & 1, u = U % 28, V = U - LAG, v = (V + 28) % 28;
      if constexpr (U < 112 && TJ[u] == 6) pinq<48 + 2 * TI[u] + s>(E[TI[u]][s]);
      if constexpr (U < 112) mf<s == 0>(D[U % (LAG + 1)], E[TJ[u] < 6 ? TI[u] : 6][s], E[TJ[u] < 6 ? TJ[u] : TI[u]][s]);
      SCHED();
      if constexpr (erat(k) >= 0) read_e(erat(k) >> 4, erat(k) & 15);
      if constexpr (s == 0 && U + WL >= NW0) read_wt(IC<U + WL>{});
      if constexpr (V >= 0 && V < 112) {
        const f32x4& d = D[V % (LAG + 1)];
        float& a = V >= 112 - LAG && s == 1 && k > KE ? sm[0] : sm[V / 28];
        if constexpr (V < 28 && wnew(v) && s == 0) pinq<16 + wti(v) / 3 * 4 + (wti(v) % 3 ? wti(v) % 3 + 1 : 0)>(wt[wti(v)]);
        if constexpr (TJ[v] < 6) { a = __builtin_fmaf(d[2 * s + 1], wt[wti(v)][2 * s + 1], __builtin_fmaf(d[2 * s], wt[wti(v)][2 * s], a)); hold(a); }
        else if constexpr (s == 0) { a = v == 0 ? d[0] * wt[wti(v)][TI[v] & 3] : __builtin_fmaf(d[0], wt[wti(v)][TI[v] & 3], a); hold(a); hold(d); }
      }
      if constexpr (V >= 0 && V < 112 && TJ[v] == 6) {
        constexpr int b = TI[v], sg = V / 28, n = (b + 1) % 7;
        if constexpr (sg == 0 && s == 0) asm volatile("v_mov_b32 %0, %1" : "=v"(ea[3][b]) : "v"(ea[2][b]));
        if constexpr (sg == 0 && s == 1) { const auto t = __builtin_amdgcn_permlane16_swap(ea[2][b], ea[3][b], false, false); ea[2][b] = t[0]; ea[3][b] = t[1]; }
        if constexpr (sg == 1 && s == 1) { ea[2][b] ^= q16; ea[3][b] ^= q16; }
        if constexpr (sg < 2 && s == 1) { hold(ea[2][b]); hold(ea[3][b]); }
        if constexpr (sg == 1 && s == 0 && n < 2) { mt[n] = (unsigned)(tag >> (32 * n)); opaque(mt[n]); }
        if constexpr (sg == 2 && s == 1 && n == 1) BST128C(((u32x4){__float_as_uint(sm[0]), mt[0], __float_as_uint(sm[1]), mt[1]}), rs_ws, wa, WS_PN - L_WT + ((bid ^ 8) * 8 + w) * 1024);
        if constexpr (sg == 2 && s == 1 && n == 2) { yo = wa * 8; opaque(yo); }
        if constexpr (sg == 2 && s == 1 && n == 3) { oo = (q16 >> 1) & 16; opaque(oo); }
        if constexpr (sg == 2 && s == 1 && n == 4) oo = pick(-1, oo, 0x8000000080000000ull);
        if constexpr (sg == 2 && s == 1 && n == 5) { bb = 0; opaque(bb); }
      }
      if constexpr (k == KE) {
        u64 t0, t1; int left = YSP;
        asm volatile(".Lchk%=:\n\ts_waitcnt vmcnt(0)\n\tv_cmp_ne_u32 vcc, %[tl], v1\n\tv_cmp_ne_u32 %[t0], %[th], v3\n\tv_cmp_ne_u32 %[t1], %[tl], v5\n\ts_or_b64 %[t0], %[t0], vcc\n\t"
                     "v_cmp_ne_u32 vcc, %[th], v7\n\ts_or_b64 %[t0], %[t0], %[t1]\n\ts_or_b64 %[t0], %[t0], vcc\n\ts_cbranch_scc0 .Lok%=\n\t"
                     "s_sleep %[slp]\n\ts_mov_b64 exec, %[t0]\n\tbuffer_load_dwordx4 v[0:3], %[yo], %[rs], %[yb] offen sc1\n\tbuffer_load_dwordx4 v[4:7], %[wa], %[rs], %[mb] offen sc1\n\t"
                     "s_mov_b64 exec, -1\n\ts_sub_u32 %[n], %[n], 1\n\ts_cbranch_scc0 .Lchk%=\n\ts_waitcnt vmcnt(0)\n.Lok%=:"
                     : "+{v[0:3]}"(yv), "+{v[4:7]}"(msg), "+v"(bb), [t0] "=&s"(t0), [t1] "=&s"(t1), [n] "+s"(left)
                     : [tl] "s"((unsigned)tag), [th] "s"((unsigned)(tag >> 32)), [yo] "v"(yo), [wa] "v"(wa), [rs] "s"(rs_ws), [yb] "s"(yb), [mb] "s"(mb), [slp] "i"(YSL) : "memory", "scc", "vcc");
        STAMP(16);
        sm[0] = __uint_as_float(msg[2]) + __uint_as_float(yv[2]); hold(sm[0]);
      }
      if constexpr (k == KE + 1) { sm[1] = __uint_as_float(msg[0]) + __uint_as_float(yv[0]); hold(sm[1]); }
      if constexpr (k == KE + 2) { sm[2] += sm[1]; hold(sm[2]); }
      if constexpr (U >= 112 && k >= KN) asm volatile("s_nop 1");
      side(K);
      if constexpr (k == 223) { TSTAMP(6); RST(7); CK_(4); }
      SCHED();
    });
    {
      STAMP(17);
      const auto t = __builtin_amdgcn_permlane32_swap(__float_as_uint(sm[2]), __float_as_uint(sm[3] + sm[0]), false, false);
      float a = row_sum(__uint_as_float(t[0]) + __uint_as_float(t[1]));
      a += __uint_as_float(__builtin_amdgcn_mov_dpp(__float_as_uint(a), 0x142, 0xf, 0xf, true));
      __builtin_amdgcn_raw_buffer_store_b16((u16)pack2(a + __uint_as_float(bb), 0.f), mkrs((char*)(gp)arg8, NB * 2), oo, (bid * 16 + w) * 2, 0);
      if (!old && (bid | w) == 0) __hip_atomic_store((gu64*)(ws + WS_EPOCH), tag, RLX_AGENT);
    }
    STAMP(18);
    RSTAMP(63);
  };
  if (w >= 4) {
    sfor<1, 6>([&](auto J) { period(IC<1>{}, J); });
    rest(IC<1>{});
    asm volatile("s_endpgm" ::: "memory");
#pragma unroll
    for (int i = 0; i < 32; ++i) RESERVE(T[i >> 2][i & 3]);
#pragma unroll
    for (int i = 0; i < 4; ++i) RESERVE(ix[i]);
    RESERVE(l16); RESERVE(lx); RESERVE(bz);
    asm volatile("" : "+s"(w), "+s"(bid), "+s"(rr), "+s"(g), "+s"(phi), "+s"(par), "+s"(wsb), "+s"(lsb), "+s"(slice), "+s"(wv));
  }
  sfor<1, 6>([&](auto J) { period(IC<0>{}, J); });
  rest(IC<0>{});
  asm volatile(".Lcode_end:");
}

extern "C" void kernel_launch(void* const* d_in, const int* in_sizes, int n_in, void* d_out, int out_size,
                              void* d_ws, size_t ws_size, hipStream_t stream) {
  k_fused<<<256, 512, 0, stream>>>((const int*)d_in[0], (const char*)d_in[1], (const u16*)d_in[2], (const char*)d_in[3],
                                   (const u16*)d_in[4], (const u16*)d_in[5], (const u16*)d_in[6], (char*)d_ws, (u16*)d_out);
}
```
